```python
import torch

def _softmax(x, axis=-1):
    return torch.softmax(x, dim=axis)
def _gelu(x):
    return torch.nn.functional.gelu(x, approximate="tanh")
def _silu(x):
    return torch.nn.functional.silu(x)
def _sigmoid(x):
    return torch.sigmoid(x)
def _layernorm(x, axis=-1, eps=1e-5):
    m = x.mean(dim=axis, keepdim=True)
    v = x.var(dim=axis, keepdim=True, unbiased=False)
    return (x - m) / torch.sqrt(v + eps)
def _scatter_add(tgt, idx, src, axis=0):
    return tgt.clone().index_add_(axis, idx, src)
def _segment_sum(src, seg, n):
    out = torch.zeros((n,) + src.shape[1:], dtype=src.dtype)
    return out.index_add_(0, seg, src)
def _pad(x, pw):
    flat = tuple(v for pair in reversed(pw) for v in pair)
    return torch.nn.functional.pad(x, flat)

kernel_name = 'parallel_sum__mha_block__mixer_channel_mlp__prompt'

def setup_inputs():

    _w = (lambda a: (lambda t: t.to(torch.bfloat16) if t.is_floating_point() else t)(torch.as_tensor(a)))
    def _A_setup():
        B, S, D, H = 1, 2048, 640, 32
        return {
            "x": _w((lambda *s: torch.randn(*s, dtype=torch.bfloat16) * 0.05)(B, S, D)),
            "wqkv": _w((lambda *s: torch.randn(*s, dtype=torch.bfloat16) * 0.05)(D, 3 * D)),
            "wo": _w((lambda *s: torch.randn(*s, dtype=torch.bfloat16) * 0.05)(D, D)),
        }
    _A_in = _A_setup()

    def _B_setup():
        B, S, D, F = 1, 2048, 640, 1536
        return {
            "x": _w((lambda *s: torch.randn(*s, dtype=torch.bfloat16) * 0.05)(B, S, D)),
            "w1": _w((lambda *s: torch.randn(*s, dtype=torch.bfloat16) * 3.2)(D, F) * (D ** -0.5)),
            "b1": _w((lambda *s: torch.randn(*s, dtype=torch.bfloat16) * 1.6)(F)),
            "w2": _w((lambda *s: torch.randn(*s, dtype=torch.bfloat16) * 0.1)(F, D) * (F ** -0.5)),
            "b2": _w((lambda *s: torch.randn(*s, dtype=torch.bfloat16) * 0.2)(D)),
        }
    _B_in = _B_setup()

    return {
        "A_x": _A_in["x"],
        "A_wqkv": _A_in["wqkv"],
        "A_wo": _A_in["wo"],
        "B_x": _B_in["x"],
        "B_w1": _B_in["w1"],
        "B_b1": _B_in["b1"],
        "B_w2": _B_in["w2"],
        "B_b2": _B_in["b2"],
    }

def reference(A_x, A_wqkv, A_wo, B_x, B_w1, B_b1, B_w2, B_b2):
    def _A_ref(x, wqkv, wo):
        B, S, D = x.shape
        H = 32
        Dh = D // H
        qkv = (lambda x, *d: x.permute(*d))((x @ wqkv).reshape(B, S, 3, H, Dh), 2, 0, 3, 1, 4)
        q, k, v = qkv[0], qkv[1], qkv[2]
        scores = torch.einsum("bhqd,bhkd->bhqk", q, k) * (Dh ** -0.5)
        p = _softmax(scores, axis=-1)
        o = torch.einsum("bhqk,bhkd->bhqd", p, v)
        return (lambda x, *d: x.permute(*d))(o, 0, 2, 1, 3).reshape(B, S, D) @ wo

    def _B_ref(x, w1, b1, w2, b2):
        y = _layernorm(x, axis=-1)
        h = _gelu(torch.einsum("bsd,df->bsf", y, w1) + b1)
        return torch.einsum("bsf,fd->bsd", h, w2) + b2

    x = A_x
    xn = _layernorm(x, axis=-1)
    return x + _A_ref(xn, A_wqkv, A_wo) + _B_ref(xn, B_w1, B_b1, B_w2, B_b2)
```

```cpp
#include <hip/hip_runtime.h>
#include <stdint.h>

typedef __attribute__((ext_vector_type(8))) __bf16 bf16x8;
typedef __attribute__((ext_vector_type(2))) __bf16 bf16x2;
typedef __attribute__((ext_vector_type(4))) float f32x4;
typedef __attribute__((ext_vector_type(2))) float f32x2;
typedef __attribute__((ext_vector_type(16))) float f32x16;
typedef __attribute__((ext_vector_type(4))) unsigned u32x4;
typedef __attribute__((ext_vector_type(2))) unsigned u32x2;
typedef short v4i16_t __attribute__((ext_vector_type(4)));
typedef __attribute__((address_space(3))) char* lds_ptr;
typedef unsigned short u16;

#define SBAR() __builtin_amdgcn_sched_barrier(0)
#define PIN(x) asm volatile("" : "+v"(x))
#define WAIT_BAR(N) asm volatile("s_waitcnt vmcnt(" #N ") lgkmcnt(0)\n\ts_barrier" ::: "memory")
#define WAIT_VM(N) asm volatile("s_waitcnt vmcnt(" #N ")" ::: "memory")
#define LGKM_BAR() asm volatile("s_waitcnt lgkmcnt(0)\n\ts_barrier" ::: "memory")

constexpr int SEQ = 2048, DM = 640, FF = 1536;
constexpr size_t WS_QK = 0;
constexpr size_t WS_V = WS_QK + (size_t)160 * SEQ * 16;
constexpr size_t WS_H = WS_V + (size_t)256 * DM * 16;
constexpr size_t WS_O = WS_H + (size_t)192 * SEQ * 16;

__device__ __forceinline__ void glds16(unsigned voff, const void* sbase, unsigned lds_base) {
    unsigned sv;
    asm volatile("s_mov_b32 %0, m0\n\ts_mov_b32 m0, %3\n\ts_nop 0\n\tglobal_load_lds_dwordx4 %1, %2\n\ts_mov_b32 m0, %0"
                 : "=&s"(sv) : "v"(voff), "s"(sbase), "s"(lds_base) : "memory");
}
__device__ __forceinline__ u32x2 tr_read(lds_ptr p) {
    return __builtin_bit_cast(u32x2, __builtin_amdgcn_ds_read_tr16_b64_v4i16((__attribute__((address_space(3))) v4i16_t*)p));
}
__device__ __forceinline__ u32x4 lds_rd128(lds_ptr p) { return *(__attribute__((address_space(3))) u32x4*)p; }
__device__ __forceinline__ f32x4 lds_rdf4(lds_ptr p) { return *(__attribute__((address_space(3))) f32x4*)p; }
__device__ __forceinline__ f32x4 mfma16(u32x4 a, u32x4 b, f32x4 c) {
    return __builtin_amdgcn_mfma_f32_16x16x32_bf16(__builtin_bit_cast(bf16x8, a), __builtin_bit_cast(bf16x8, b), c, 0, 0, 0);
}
__device__ __forceinline__ f32x16 mfma32(u32x4 a, u32x4 b, f32x16 c) {
    return __builtin_amdgcn_mfma_f32_32x32x16_bf16(__builtin_bit_cast(bf16x8, a), __builtin_bit_cast(bf16x8, b), c, 0, 0, 0);
}
__device__ __forceinline__ float dot2(unsigned a, unsigned b, float c) {
    return __builtin_amdgcn_fdot2_f32_bf16(__builtin_bit_cast(bf16x2, a), __builtin_bit_cast(bf16x2, b), c, false);
}
__device__ __forceinline__ unsigned pk2(float lo, float hi) {
    bf16x2 v = {(__bf16)lo, (__bf16)hi};
    return __builtin_bit_cast(unsigned, v);
}
__device__ __forceinline__ float bf_lo(unsigned u) { return __uint_as_float(u << 16); }
__device__ __forceinline__ float bf_hi(unsigned u) { return __uint_as_float(u & 0xffff0000u); }
__device__ __forceinline__ float sum_groups(float v) {
    auto a = __builtin_amdgcn_permlane16_swap(__float_as_uint(v), __float_as_uint(v), false, false);
    v = __uint_as_float(a[0]) + __uint_as_float(a[1]);
    auto b = __builtin_amdgcn_permlane32_swap(__float_as_uint(v), __float_as_uint(v), false, false);
    return __uint_as_float(b[0]) + __uint_as_float(b[1]);
}
constexpr unsigned ONES2 = 0x3f803f80u;

constexpr int K1_STAGE = 49152, K1_WOFF = 16384;
constexpr int K1_ROWST = 3 * K1_STAGE;
constexpr int K1_COLS = K1_ROWST + 1024;
constexpr int K1_LDS = K1_COLS + 4096;

__device__ __forceinline__ float gelu_tanh(float u) {
    const float c1 = -2.0f * 0.7978845608028654f * 1.4426950408889634f;
    const float c2 = c1 * 0.044715f;
    float t = u * __builtin_fmaf(u * u, c2, c1);
    float e = __builtin_amdgcn_exp2f(t);
    return u * __builtin_amdgcn_rcpf(1.0f + e);
}

template <int NT, int WN, bool ISV, bool MLP>
__device__ __forceinline__ void k1_body(lds_ptr lds, unsigned lds0, const char* xg, const char* wg, int tA, int tB,
                                        const u16* x, const u16* b1, char* ws, int mb, int w, int lane) {
    constexpr int NSUB = MLP ? 3 : 4;
    constexpr int PITCH = MLP ? FF * 2 : 3 * DM * 2;
    const int wm = w & 3;
    const int g = lane >> 4, l15 = lane & 15;
    unsigned xoff0, xoff1, woff;
    {
        int r0 = 16 * w + (lane >> 3), r1 = r0 + 8;
        xoff0 = r0 * (DM * 2) + (((lane & 7) ^ ((r0 >> 1) & 7)) * 16);
        xoff1 = r1 * (DM * 2) + (((lane & 7) ^ ((r1 >> 1) & 7)) * 16);
        int q8 = lane >> 3, pos = lane & 7;
        int a = (pos >> 1) ^ (((q8 >> 1) & 1) | ((w & 1) << 1));
        woff = (8 * w + q8) * PITCH + a * 32 + (pos & 1) * 16;
    }
    auto issue = [&](int s, int slot) {
        const char* xs = xg + s * 128;
        const char* wsrc = wg + (size_t)s * 64 * PITCH;
        unsigned d = lds0 + slot * K1_STAGE;
        glds16(xoff0, xs, d + (2 * w) * 1024);
        glds16(xoff1, xs, d + (2 * w + 1) * 1024);
        unsigned dw = d + K1_WOFF + w * NSUB * 1024;
        if constexpr (MLP) {
            glds16(woff, wsrc + tA * 32, dw);
            glds16(woff, wsrc + tA * 32 + 128, dw + 1024);
            glds16(woff, wsrc + tA * 32 + 256, dw + 2048);
        } else {
            glds16(woff, wsrc + tA * 32, dw);
            glds16(woff, wsrc + tA * 32 + 128, dw + 1024);
            glds16(woff, wsrc + tB * 32, dw + 2048);
            glds16(woff, wsrc + tB * 32 + 128, dw + 3072);
        }
    };
    unsigned xb[2], wb[4];
    {
        int t = wm * 32 + l15;
        int f = (t >> 1) & 7;
        xb[0] = t * 128 + ((g ^ f) * 16);
        xb[1] = t * 128 + (((4 + g) ^ f) * 16);
        int q = (lane >> 2) & 3, p = lane & 3;
        int phi = ((q >> 1) & 1) | ((g & 1) << 1);
#pragma unroll
        for (int a = 0; a < 4; ++a) wb[a] = K1_WOFF + g * NSUB * 1024 + q * 128 + ((a ^ phi) * 32) + p * 8;
    }
    f32x4 acc[2][NT];
#pragma unroll
    for (int m = 0; m < 2; ++m)
#pragma unroll
        for (int i = 0; i < NT; ++i) acc[m][i] = f32x4{0.f, 0.f, 0.f, 0.f};
    float csum[NT];
#pragma unroll
    for (int i = 0; i < NT; ++i) csum[i] = 0.f;
    float rs = 0.f, rq = 0.f;
    u32x4 xf0[2], wf0[NT], xf1[2], wf1[NT];

    auto rd = [&](int slot, int ks, u32x4* xf, u32x4* wf) {
        lds_ptr sp = lds + slot * K1_STAGE;
        xf[0] = lds_rd128(sp + xb[ks]);
        xf[1] = lds_rd128(sp + xb[ks] + 2048);
#pragma unroll
        for (int i = 0; i < NT; ++i) {
            const int tl = MLP ? 6 * WN + i : i;
            const int sub = MLP ? (tl >> 2) : (WN * 2 + (i >> 2));
            lds_ptr p = sp + wb[tl & 3] + ks * 4 * NSUB * 1024 + sub * 1024;
            u32x2 lo = tr_read(p), hi = tr_read(p + 512);
            wf[i] = u32x4{lo[0], lo[1], hi[0], hi[1]};
        }
    };
    auto comp = [&](int kk, const u32x4* xf, const u32x4* wf) {
#pragma unroll
        for (int i = 0; i < NT; ++i) {
            if constexpr (ISV) {
                acc[0][i] = mfma16(xf[0], wf[i], acc[0][i]);
                acc[1][i] = mfma16(xf[1], wf[i], acc[1][i]);
            } else {
                acc[0][i] = mfma16(wf[i], xf[0], acc[0][i]);
                acc[1][i] = mfma16(wf[i], xf[1], acc[1][i]);
            }
        }
#pragma unroll
        for (int e = 0; e < 4; ++e) {
            rs = dot2(xf[WN][e], ONES2, rs);
            rq = dot2(xf[WN][e], xf[WN][e], rq);
        }
        if ((kk & 3) == wm) {
#pragma unroll
            for (int i = 0; i < NT; ++i)
#pragma unroll
                for (int e = 0; e < 4; ++e) csum[i] = dot2(wf[i][e], ONES2, csum[i]);
        }
    };

    issue(0, 0);
    issue(1, 1);
    issue(2, 2);
    if constexpr (MLP) { WAIT_BAR(10); } else { WAIT_BAR(12); }
    rd(0, 0, xf0, wf0);
    int slot = 0;
    for (int s = 0; s < 10; ++s) {
        rd(slot, 1, xf1, wf1);
        comp(2 * s, xf0, wf0);
        const int nslot = slot == 2 ? 0 : slot + 1;
        if (s < 8) {
            if constexpr (MLP) { WAIT_BAR(5); } else { WAIT_BAR(6); }
        } else {
            WAIT_BAR(0);
        }
        if (s < 7) issue(s + 3, slot);
        if (s < 9) rd(nslot, 0, xf0, wf0);
        comp(2 * s + 1, xf1, wf1);
        slot = nslot;
    }
    rs = sum_groups(rs);
    rq = sum_groups(rq);
    {
        const int tl = wm * 32 + WN * 16 + l15;
        float mu = rs * (1.0f / DM);
        float var = __builtin_fmaf(-mu, mu, rq * (1.0f / DM));
        if (!(var >= mu * mu * (1.0f / 256.0f))) {
            const u16* xr = x + (size_t)(mb * 128 + tl) * DM;
            float a = 0.f;
            for (int k = 0; k < DM; ++k) {
                float d = __uint_as_float((unsigned)xr[k] << 16) - mu;
                a = __builtin_fmaf(d, d, a);
            }
            var = a * (1.0f / DM);
        }
        float r = 1.0f / __builtin_sqrtf(var + 1e-5f);
        if constexpr (MLP) {
            float v2 = var * r * r;
            r = r / __builtin_sqrtf(v2 + 1e-5f);
        }
        if (g == 0) *(__attribute__((address_space(3))) f32x2*)(lds + K1_ROWST + tl * 8) = f32x2{mu, r};
    }
#pragma unroll
    for (int i = 0; i < NT; ++i) {
        float c = sum_groups(csum[i]);
        if (g == 0) *(__attribute__((address_space(3))) float*)(lds + K1_COLS + wm * 1024 + (WN * 8 + i) * 64 + l15 * 4) = c;
    }
    LGKM_BAR();
    const int tok0 = mb * 128 + wm * 32;
    if constexpr (ISV) {
        f32x2 st[2][4];
#pragma unroll
        for (int m = 0; m < 2; ++m) {
            f32x4 a = lds_rdf4(lds + K1_ROWST + (wm * 32 + m * 16 + g * 4) * 8);
            f32x4 b = lds_rdf4(lds + K1_ROWST + (wm * 32 + m * 16 + g * 4) * 8 + 16);
            st[m][0] = f32x2{a[0], a[1]}; st[m][1] = f32x2{a[2], a[3]}; st[m][2] = f32x2{b[0], b[1]}; st[m][3] = f32x2{b[2], b[3]};
        }
        const int to = (tok0 >> 3) + ((g & 1) * 2 + (g >> 1));
        char* vp = ws + WS_V + ((size_t)to * DM + (tA - 80) * 16 + l15) * 16;
#pragma unroll
        for (int i = 0; i < NT; ++i) {
            lds_ptr cp = lds + K1_COLS + (WN * 8 + i) * 64 + l15 * 4;
            typedef __attribute__((address_space(3))) float* lf;
            float cs = (*(lf)cp + *(lf)(cp + 1024)) + (*(lf)(cp + 2048) + *(lf)(cp + 3072));
            unsigned pkd[2][2];
#pragma unroll
            for (int m = 0; m < 2; ++m) {
                float v[4];
#pragma unroll
                for (int r = 0; r < 4; ++r) v[r] = st[m][r][1] * __builtin_fmaf(-st[m][r][0], cs, acc[m][i][r]);
                pkd[m][0] = pk2(v[0], v[1]);
                pkd[m][1] = pk2(v[2], v[3]);
            }
            auto s0 = __builtin_amdgcn_permlane16_swap(pkd[0][0], pkd[1][0], false, false);
            auto s1 = __builtin_amdgcn_permlane16_swap(pkd[0][1], pkd[1][1], false, false);
            *(u32x4*)(vp + i * 256) = u32x4{s0[0], s1[0], s0[1], s1[1]};
        }
    } else {
        f32x2 st[2];
#pragma unroll
        for (int m = 0; m < 2; ++m) st[m] = *(__attribute__((address_space(3))) f32x2*)(lds + K1_ROWST + (wm * 32 + m * 16 + l15) * 8);
        const int T0 = MLP ? tA + 6 * WN : (WN ? tB : tA);
        char* op = ws + (MLP ? WS_H : WS_QK) + ((size_t)(2 * T0 + (g >> 1)) * SEQ + tok0 + (g & 1) * 16 + l15) * 16;
#pragma unroll
        for (int i = 0; i < NT; ++i) {
            lds_ptr cp = lds + K1_COLS + (WN * 8 + i) * 64 + g * 16;
            f32x4 cs = (lds_rdf4(cp) + lds_rdf4(cp + 1024)) + (lds_rdf4(cp + 2048) + lds_rdf4(cp + 3072));
            float bias[4];
            float sc = 1.0f;
            if constexpr (MLP) {
                u32x2 bb = *(const u32x2*)(b1 + (T0 + i) * 16 + g * 4);
                bias[0] = bf_lo(bb[0]); bias[1] = bf_hi(bb[0]); bias[2] = bf_lo(bb[1]); bias[3] = bf_hi(bb[1]);
            } else {
                sc = (T0 + i < 40) ? 0.22360679774997896f * 1.4426950408889634f : 1.0f;
            }
            unsigned pkd[2][2];
#pragma unroll
            for (int m = 0; m < 2; ++m) {
                float v[4];
                const float rr = st[m][1] * sc;
#pragma unroll
                for (int r = 0; r < 4; ++r) {
                    float u = __builtin_fmaf(-st[m][0], cs[r], acc[m][i][r]);
                    if constexpr (MLP) v[r] = gelu_tanh(__builtin_fmaf(u, rr, bias[r]));
                    else v[r] = u * rr;
                }
                pkd[m][0] = pk2(v[0], v[1]);
                pkd[m][1] = pk2(v[2], v[3]);
            }
            auto s0 = __builtin_amdgcn_permlane16_swap(pkd[0][0], pkd[1][0], false, false);
            auto s1 = __builtin_amdgcn_permlane16_swap(pkd[0][1], pkd[1][1], false, false);
            *(u32x4*)(op + (size_t)i * 2 * SEQ * 16) = u32x4{s0[0], s1[0], s0[1], s1[1]};
        }
    }
}

__global__ __launch_bounds__(512, 2) void k1_qkv_mlp1(const u16* __restrict__ x, const u16* __restrict__ wqkv, const u16* __restrict__ w1,
                                                      const u16* __restrict__ b1, char* __restrict__ ws) {
    __shared__ __attribute__((aligned(1024))) char smem[K1_LDS];
    lds_ptr lds = (lds_ptr)smem;
    const unsigned lds0 = (unsigned)(uintptr_t)lds;
    const int lane = threadIdx.x & 63;
    const int w = __builtin_amdgcn_readfirstlane(threadIdx.x >> 6);
    const int b = blockIdx.x, xcd = b & 7, idx = b >> 3;
    const int mb = (xcd & 1) * 8 + (idx & 7), cb = idx >> 3, j = 2 * (xcd >> 1) + (cb & 1);
    const char* xg = (const char*)x + (size_t)mb * 128 * DM * 2;
    if (cb >= 2) {
        if (w < 4) k1_body<6, 0, false, true>(lds, lds0, xg, (const char*)w1, 12 * j, 0, x, b1, ws, mb, w, lane);
        else k1_body<6, 1, false, true>(lds, lds0, xg, (const char*)w1, 12 * j, 0, x, b1, ws, mb, w, lane);
    } else {
        const int tA = j < 5 ? 80 + 8 * j : 56 + 8 * (j - 5), tB = 7 * j;
        if (w >= 4) k1_body<7, 1, false, false>(lds, lds0, xg, (const char*)wqkv, tA, tB, x, b1, ws, mb, w, lane);
        else if (j < 5) k1_body<8, 0, true, false>(lds, lds0, xg, (const char*)wqkv, tA, tB, x, b1, ws, mb, w, lane);
        else k1_body<8, 0, false, false>(lds, lds0, xg, (const char*)wqkv, tA, tB, x, b1, ws, mb, w, lane);
    }
}

constexpr int K2_SLOT = 22528, K2_VOFF = 12288, K2_NSLOT = 6;
constexpr int K2_ONES = K2_NSLOT * K2_SLOT;
constexpr int K2_FLAG = K2_ONES + 10240;
constexpr int K2_LDS = K2_FLAG + 64;

template <int MODE>
__device__ __forceinline__ void k2_pass(lds_ptr lds, unsigned lds0, const char* ws, int w, const unsigned* doff, const unsigned* dstr,
                                        unsigned kmain0, unsigned kx0, unsigned vb0, unsigned vinc, u32x4 q0, u32x4 q1, bool first,
                                        float& mref, f32x16& o) {
    auto issue = [&](int c) {
        const int slot = c >= K2_NSLOT ? c - K2_NSLOT : c;
        unsigned d = lds0 + slot * K2_SLOT + w * 1024;
        glds16(doff[0], ws + (size_t)c * dstr[0], d);
        glds16(doff[1], ws + (size_t)c * dstr[1], d + 8192);
        if (w < 6) glds16(doff[2], ws + (size_t)c * dstr[2], d + 16384);
    };
#pragma unroll
    for (int c = 0; c < K2_NSLOT; ++c) issue(c);
    if (w < 6) { WAIT_VM(15); } else { WAIT_VM(10); }
    LGKM_BAR();
    asm volatile("" : "+v"(q0), "+v"(q1));
    unsigned km = kmain0, kx = kx0, vb = vb0;
    u32x4 kf0 = lds_rd128(lds + km), kf1 = lds_rd128(lds + kx);
    f32x16 negm;
    f32x16 zero16;
#pragma unroll
    for (int r = 0; r < 16; ++r) zero16[r] = 0.f;
    if constexpr (MODE == 0) {
        if (first) {
            f32x16 s = mfma32(kf0, q0, zero16);
            s = mfma32(kf1, q1, s);
            float m = s[0];
#pragma unroll
            for (int r = 1; r < 16; ++r) m = __builtin_fmaxf(m, s[r]);
            auto rr = __builtin_amdgcn_permlane32_swap(__float_as_uint(m), __float_as_uint(m), false, false);
            mref = __builtin_fmaxf(__uint_as_float(rr[0]), __uint_as_float(rr[1]));
        }
#pragma unroll
        for (int r = 0; r < 16; ++r) negm[r] = -mref;
    } else {
        negm = zero16;
    }
    PIN(negm);
    float mx = -__builtin_inff();
    f32x16 sc = mfma32(kf0, q0, negm);
    sc = mfma32(kf1, q1, sc);
    kf0 = lds_rd128(lds + km + 512);
    kf1 = lds_rd128(lds + kx + 512);
    o = zero16;
    for (int c = 0; c < 8; ++c) {
        const unsigned step = (c == K2_NSLOT - 1) ? (unsigned)(-(K2_NSLOT - 1) * K2_SLOT) : (unsigned)K2_SLOT;
        const unsigned kmn = km + step, kxn = kx + step;
#pragma unroll
        for (int tt = 0; tt < 8; ++tt) {
            if (tt == 6) {
                if (c == 0 || c == 5) { WAIT_BAR(0); }
                else if (c == 2) { LGKM_BAR(); issue(6); issue(7); }
            }
            f32x16 sn = mfma32(kf0, q0, negm);
            sn = mfma32(kf1, q1, sn);
            if (tt < 6) {
                kf0 = lds_rd128(lds + km + (tt + 2) * 512);
                kf1 = lds_rd128(lds + kx + (tt + 2) * 512);
            } else {
                kf0 = lds_rd128(lds + kmn + (tt - 6) * 512);
                kf1 = lds_rd128(lds + kxn + (tt - 6) * 512);
            }
            if constexpr (MODE == 0) {
                u32x4 vf0 = lds_rd128(lds + vb + tt * 1280);
                u32x4 vf1 = lds_rd128(lds + vb + tt * 1280 + 640);
                u32x4 p0, p1;
#pragma unroll
                for (int r = 0; r < 4; ++r) p0[r] = pk2(__builtin_amdgcn_exp2f(sc[2 * r]), __builtin_amdgcn_exp2f(sc[2 * r + 1]));
                o = mfma32(vf0, p0, o);
#pragma unroll
                for (int r = 0; r < 4; ++r) p1[r] = pk2(__builtin_amdgcn_exp2f(sc[8 + 2 * r]), __builtin_amdgcn_exp2f(sc[9 + 2 * r]));
                o = mfma32(vf1, p1, o);
            } else {
#pragma unroll
                for (int r = 0; r < 16; ++r) mx = __builtin_fmaxf(mx, sc[r]);
            }
            sc = sn;
        }
        km = kmn;
        kx = kxn;
        vb += (c == K2_NSLOT - 1) ? (unsigned)(-(K2_NSLOT - 1)) * vinc : vinc;
    }
    if constexpr (MODE == 1) {
        auto rr = __builtin_amdgcn_permlane32_swap(__float_as_uint(mx), __float_as_uint(mx), false, false);
        mref = __builtin_fmaxf(__uint_as_float(rr[0]), __uint_as_float(rr[1]));
    }
}

__global__ __launch_bounds__(512, 2) void k2_attn(char* __restrict__ ws) {
    __shared__ __attribute__((aligned(1024))) char smem[K2_LDS];
    lds_ptr lds = (lds_ptr)smem;
    const unsigned lds0 = (unsigned)(uintptr_t)lds;
    const int tid = threadIdx.x, lane = tid & 63, r32 = lane & 31, hi = lane >> 5;
    const int w = __builtin_amdgcn_readfirstlane(tid >> 6);
    const int b = blockIdx.x, xcd = b & 7, idx = b >> 3;
    const int h = xcd * 4 + (idx >> 3), qb = idx & 7;
    const int odd = h & 1, plane0 = 5 * (h >> 1) + 2 * odd;
    const int mp = odd, xp = odd ? 0 : 2;
    const int tok = qb * 256 + w * 32 + r32;
    u32x4 q0, q1;
    {
        const char* qp0 = ws + WS_QK + ((size_t)(plane0 + mp + hi) * SEQ + tok) * 16;
        const char* qp1 = ws + WS_QK + ((size_t)(plane0 + xp) * SEQ + tok) * 16;
        asm volatile("global_load_dwordx4 %0, %1, off" : "=v"(q0) : "v"(qp0) : "memory");
        asm volatile("global_load_dwordx4 %0, %1, off" : "=v"(q1) : "v"(qp1) : "memory");
    }
    for (int i = tid; i < 10240 / 16; i += 512) *(__attribute__((address_space(3))) u32x4*)(lds + K2_ONES + i * 16) = u32x4{ONES2, ONES2, ONES2, ONES2};
    if (tid == 0) *(__attribute__((address_space(3))) unsigned*)(lds + K2_FLAG) = 0u;
    unsigned doff[3], dstr[3];
#pragma unroll
    for (int i = 0; i < 3; ++i) {
        const int n = w + 8 * i;
        if (n < 12) {
            doff[i] = (unsigned)WS_QK + ((80 + plane0 + (n >> 2)) * SEQ + (n & 3) * 64 + lane) * 16;
            dstr[i] = 256 * 16;
        } else {
            const int u = (n - 12) * 64 + lane, to = u / 20, d = u - to * 20;
            doff[i] = (unsigned)WS_V + (to * DM + 20 * h + d) * 16;
            dstr[i] = 32 * DM * 16;
        }
    }
    const int kap = (r32 & 19) | ((r32 & 4) << 1) | ((r32 & 8) >> 1);
    const unsigned kmain0 = (mp + hi) * 4096 + kap * 16, kx0 = xp * 4096 + kap * 16;
    unsigned vb0, vinc;
    if (r32 >= 20 && r32 < 28) { vb0 = K2_ONES + hi * 320 + 128; vinc = 0; }
    else { vb0 = K2_VOFF + hi * 320 + (r32 < 20 ? r32 : 16) * 16; vinc = K2_SLOT; }
    WAIT_VM(0);
    asm volatile("" : "+v"(q0), "+v"(q1));
    if (hi) q1 = u32x4{0u, 0u, 0u, 0u};
    else if (odd) { q1[0] = 0u; q1[1] = 0u; }
    else { q1[2] = 0u; q1[3] = 0u; }

    float mref = 0.f;
    f32x16 o;
    k2_pass<0>(lds, lds0, ws, w, doff, dstr, kmain0, kx0, vb0, vinc, q0, q1, true, mref, o);
    float l = hi ? o[8] : o[12];
    {
        bool ok = l <= 1.2676506e30f;
#pragma unroll
        for (int r = 0; r < 12; ++r) ok = ok && (__builtin_fabsf(o[r]) < __builtin_inff());
        if (!ok) *(__attribute__((address_space(3))) unsigned*)(lds + K2_FLAG) = 1u;
        LGKM_BAR();
        const unsigned fl = __builtin_amdgcn_readfirstlane(*(__attribute__((address_space(3))) unsigned*)(lds + K2_FLAG));
        if (fl) {
            k2_pass<1>(lds, lds0, ws, w, doff, dstr, kmain0, kx0, vb0, vinc, q0, q1, false, mref, o);
            LGKM_BAR();
            k2_pass<0>(lds, lds0, ws, w, doff, dstr, kmain0, kx0, vb0, vinc, q0, q1, false, mref, o);
            l = hi ? o[8] : o[12];
        }
    }
    const float inv = 1.0f / l;
#pragma unroll
    for (int qd = 0; qd < 3; ++qd) {
        const int col = 20 * h + 8 * qd + 4 * hi;
        u32x2 v = {pk2(o[4 * qd] * inv, o[4 * qd + 1] * inv), pk2(o[4 * qd + 2] * inv, o[4 * qd + 3] * inv)};
        if (qd < 2 || hi == 0) *(u32x2*)(ws + WS_O + ((size_t)(col >> 3) * SEQ + tok) * 16 + ((col >> 2) & 1) * 8) = v;
    }
}

constexpr int K3_LDS = 102400;

__global__ __launch_bounds__(512, 2) void k3_out(const u16* __restrict__ x, const u16* __restrict__ wo, const u16* __restrict__ w2,
                                                 const u16* __restrict__ b2, const char* __restrict__ ws, u16* __restrict__ out) {
    __shared__ __attribute__((aligned(1024))) char smem[K3_LDS];
    lds_ptr lds = (lds_ptr)smem;
    const unsigned lds0 = (unsigned)(uintptr_t)lds;
    const int tid = threadIdx.x, lane = tid & 63, g = lane >> 4, l15 = lane & 15;
    const int w = __builtin_amdgcn_readfirstlane(tid >> 6);
    const int b = blockIdx.x, xcd = b & 7, idx = b >> 3;
    const int mb = xcd * 4 + (idx & 3), nb = idx >> 2;
    const int mh = w & 1, kq = w >> 1;
    f32x4 acc[2][5];
#pragma unroll
    for (int m = 0; m < 2; ++m)
#pragma unroll
        for (int i = 0; i < 5; ++i) acc[m][i] = f32x4{0.f, 0.f, 0.f, 0.f};
    const int q = (lane >> 2) & 3, p = lane & 3;
    const unsigned wb0 = g * 1280 + ((q ^ ((g & 1) << 2)) * 32) + p * 8, wb1 = wb0 ^ 128;
    const int tok0 = mb * 64 + mh * 32;
    for (int ph = 0; ph < 4; ++ph) {
        const int nks = ph == 0 ? 5 : 4;
        const char* wsrc = (ph == 0 ? (const char*)wo : (const char*)w2 + (size_t)(ph - 1) * 512 * DM * 2) + nb * 160;
        const char* asrc = ws + (ph == 0 ? WS_O : WS_H + (size_t)(ph - 1) * 64 * SEQ * 16);
        const int ninstr = nks * 20;
        if (ph) LGKM_BAR();
        for (int n = w; n < ninstr; n += 8) {
            const int u = n * 64 + lane, ko = u / 80, r = u - ko * 80, t = r >> 4, pos = (r >> 1) & 7, hf = r & 1;
            const int row = ko * 8 + (pos ^ ((ko & 1) << 2));
            glds16(row * (DM * 2) + t * 32 + hf * 16, wsrc, lds0 + n * 1024);
        }
        u32x4 af[5][2];
#pragma unroll
        for (int ks = 0; ks < 5; ++ks)
            if (ks < nks) {
#pragma unroll
                for (int m = 0; m < 2; ++m)
                    af[ks][m] = *(const u32x4*)(asrc + ((size_t)((kq * nks + ks) * 4 + g) * SEQ + tok0 + m * 16 + l15) * 16);
            }
        WAIT_BAR(0);
#pragma unroll
        for (int ks = 0; ks < 5; ++ks)
            if (ks < nks) {
                lds_ptr sp = lds + (kq * nks + ks) * 5120;
#pragma unroll
                for (int i = 0; i < 5; ++i) {
                    u32x2 lo = tr_read(sp + wb0 + i * 256), hi2 = tr_read(sp + wb1 + i * 256);
                    u32x4 wf = u32x4{lo[0], lo[1], hi2[0], hi2[1]};
                    acc[0][i] = mfma16(wf, af[ks][0], acc[0][i]);
                    acc[1][i] = mfma16(wf, af[ks][1], acc[1][i]);
                }
            }
    }
    LGKM_BAR();
#pragma unroll
    for (int m = 0; m < 2; ++m)
#pragma unroll
        for (int i = 0; i < 5; ++i)
            *(__attribute__((address_space(3))) f32x4*)(lds + ((kq * 20 + mh * 10 + m * 5 + i) * 64 + lane) * 16) = acc[m][i];
    LGKM_BAR();
    for (int t = w; t < 20; t += 8) {
        f32x4 s = (lds_rdf4(lds + (t * 64 + lane) * 16) + lds_rdf4(lds + ((20 + t) * 64 + lane) * 16)) +
                  (lds_rdf4(lds + ((40 + t) * 64 + lane) * 16) + lds_rdf4(lds + ((60 + t) * 64 + lane) * 16));
        const int mt = t / 5, i = t - mt * 5;
        const int tk = mb * 64 + mt * 16 + l15, col = nb * 80 + i * 16 + g * 4;
        u32x2 xv = *(const u32x2*)(x + (size_t)tk * DM + col);
        u32x2 bv = *(const u32x2*)(b2 + col);
        u32x2 r;
        r[0] = pk2(s[0] + bf_lo(xv[0]) + bf_lo(bv[0]), s[1] + bf_hi(xv[0]) + bf_hi(bv[0]));
        r[1] = pk2(s[2] + bf_lo(xv[1]) + bf_lo(bv[1]), s[3] + bf_hi(xv[1]) + bf_hi(bv[1]));
        *(u32x2*)(out + (size_t)tk * DM + col) = r;
    }
}

extern "C" void kernel_launch(void* const* d_in, const int* in_sizes, int n_in, void* d_out, int out_size, void* d_ws, size_t ws_size,
                              hipStream_t stream) {
    const u16* x = (const u16*)d_in[0];
    const u16* wqkv = (const u16*)d_in[1];
    const u16* wo = (const u16*)d_in[2];
    const u16* w1 = (const u16*)d_in[4];
    const u16* b1 = (const u16*)d_in[5];
    const u16* w2 = (const u16*)d_in[6];
    const u16* b2 = (const u16*)d_in[7];
    char* ws = (char*)d_ws;
    k1_qkv_mlp1<<<256, 512, 0, stream>>>(x, wqkv, w1, b1, ws);
    k2_attn<<<256, 512, 0, stream>>>(ws);
    k3_out<<<256, 512, 0, stream>>>(x, wo, w2, b2, ws, (u16*)d_out);
}
```

```cpp
#include <hip/hip_runtime.h>
#include <stdint.h>

typedef __attribute__((ext_vector_type(8))) __bf16 bf16x8;
typedef __attribute__((ext_vector_type(2))) __bf16 bf16x2;
typedef __attribute__((ext_vector_type(4))) float f32x4;
typedef __attribute__((ext_vector_type(2))) float f32x2;
typedef __attribute__((ext_vector_type(16))) float f32x16;
typedef __attribute__((ext_vector_type(4))) unsigned u32x4;
typedef __attribute__((ext_vector_type(2))) unsigned u32x2;
typedef short v4i16_t __attribute__((ext_vector_type(4)));
typedef __attribute__((address_space(3))) char* lds_ptr;
typedef unsigned short u16;
template <int V> struct IC { static constexpr int v = V; };

#define SBAR() __builtin_amdgcn_sched_barrier(0)
#define PIN(x) asm volatile("" : "+v"(x))
#define WAIT_BAR(N) asm volatile("s_waitcnt vmcnt(" #N ") lgkmcnt(0)\n\ts_barrier" ::: "memory")
#define WAIT_VM(N) asm volatile("s_waitcnt vmcnt(" #N ")" ::: "memory")
#define LGKM_BAR() asm volatile("s_waitcnt lgkmcnt(0)\n\ts_barrier" ::: "memory")

#define PROF_LDS 0
#define STAMP(lds, base, i) do {} while (0)
#define STAMP_DUMP(ws, lds, base, kid, n) do {} while (0)
constexpr int SEQ = 2048, DM = 640, FF = 1536;
constexpr size_t WS_QK = 0;
constexpr size_t WS_V = WS_QK + (size_t)160 * SEQ * 16;
constexpr size_t WS_O = WS_V + (size_t)256 * DM * 16;
constexpr size_t WS_H = WS_O + (size_t)80 * SEQ * 16;
constexpr size_t WS_WT = WS_H + (size_t)192 * SEQ * 16;
constexpr size_t WS_P = WS_WT + (size_t)272 * DM * 16;

__device__ __forceinline__ void glds16(unsigned voff, const void* sbase, unsigned lds_base) {
    unsigned sv;
    asm volatile("s_mov_b32 %0, m0\n\ts_mov_b32 m0, %3\n\ts_nop 0\n\tglobal_load_lds_dwordx4 %1, %2\n\ts_mov_b32 m0, %0"
                 : "=&s"(sv) : "v"(voff), "s"(sbase), "s"(lds_base) : "memory");
}
__device__ __forceinline__ u32x2 tr_read(lds_ptr p) {
    return __builtin_bit_cast(u32x2, __builtin_amdgcn_ds_read_tr16_b64_v4i16((__attribute__((address_space(3))) v4i16_t*)p));
}
__device__ __forceinline__ u32x4 lds_rd128(lds_ptr p) { return *(__attribute__((address_space(3))) u32x4*)p; }
__device__ __forceinline__ f32x4 lds_rdf4(lds_ptr p) { return *(__attribute__((address_space(3))) f32x4*)p; }
__device__ __forceinline__ f32x2 lds_rdf2(lds_ptr p) { return *(__attribute__((address_space(3))) f32x2*)p; }
__device__ __forceinline__ float lds_rdf(lds_ptr p) { return *(__attribute__((address_space(3))) float*)p; }
__device__ __forceinline__ void lds_wrf4(lds_ptr p, f32x4 v) { *(__attribute__((address_space(3))) f32x4*)p = v; }
__device__ __forceinline__ void lds_wrf2(lds_ptr p, f32x2 v) { *(__attribute__((address_space(3))) f32x2*)p = v; }
__device__ __forceinline__ void lds_wrf(lds_ptr p, float v) { *(__attribute__((address_space(3))) float*)p = v; }
__device__ __forceinline__ f32x4 mfma16(u32x4 a, u32x4 b, f32x4 c) {
    return __builtin_amdgcn_mfma_f32_16x16x32_bf16(__builtin_bit_cast(bf16x8, a), __builtin_bit_cast(bf16x8, b), c, 0, 0, 0);
}
__device__ __forceinline__ f32x16 mfma32(u32x4 a, u32x4 b, f32x16 c) {
    return __builtin_amdgcn_mfma_f32_32x32x16_bf16(__builtin_bit_cast(bf16x8, a), __builtin_bit_cast(bf16x8, b), c, 0, 0, 0);
}
__device__ __forceinline__ unsigned pk2(float lo, float hi) {
    bf16x2 v = {(__bf16)lo, (__bf16)hi};
    return __builtin_bit_cast(unsigned, v);
}
__device__ __forceinline__ float bf_lo(unsigned u) { return __uint_as_float(u << 16); }
__device__ __forceinline__ float bf_hi(unsigned u) { return __uint_as_float(u & 0xffff0000u); }
constexpr unsigned ONES2 = 0x3f803f80u;

constexpr int K1_STAGE = 45056, K1_QOFF = 16384, K1_MOFF = 32768;
constexpr int K1_ROWST = 3 * K1_STAGE;
constexpr int K1_COLS = K1_ROWST + 2048;
constexpr int K1_TR = K1_COLS + 2048;
constexpr int K1_LDS = K1_TR + 16 * 1024;

__device__ __forceinline__ f32x2 gelu_tanh2(f32x2 u) {
    const float c1 = -2.0f * 0.7978845608028654f * 1.4426950408889634f;
    const float c2 = c1 * 0.044715f;
    f32x2 t = u * __builtin_elementwise_fma(u * u, f32x2{c2, c2}, f32x2{c1, c1});
    f32x2 d = f32x2{__builtin_amdgcn_exp2f(t[0]), __builtin_amdgcn_exp2f(t[1])} + f32x2{1.0f, 1.0f};
    return u * f32x2{__builtin_amdgcn_rcpf(d[0]), __builtin_amdgcn_rcpf(d[1])};
}

struct K1Dma { unsigned vx, vq, vm0, vm1, tv; const char *xg, *qg, *mg, *tsrc0, *tsrc1; unsigned dst; };

template <int TYPE, int WM>
__device__ __forceinline__ void k1_loop(lds_ptr lds, const K1Dma& dm, const unsigned (&xa)[2][2], const unsigned (&wa)[2][4],
                                        f32x4 (&acc)[4][4], f32x4 (&cs)[4], f32x4 (&st)[4]) {
    constexpr int NT = TYPE == 2 ? 3 : 4;
    constexpr int HI = TYPE == 2 ? 768 : 512;
    constexpr int NR = 4 + 2 * NT;
    const u32x4 ones = {ONES2, ONES2, ONES2, ONES2};
    u32x4 xf[2][4];
    u32x2 wl[2][4], wh[2][4];
    auto piece = [&](int j, int s, unsigned so) {
        const unsigned d = dm.dst + so + j * 8192;
        if (j == 0) glds16(dm.vx, dm.xg + s * 128, d);
        if (j == 1) glds16(dm.vx, dm.xg + s * 128 + 64 * DM * 2, d);
        if (j == 2) glds16(dm.vq, dm.qg + (size_t)s * (64 * 3 * DM * 2), d);
        if (j == 3) glds16(dm.vq, dm.qg + (size_t)s * (64 * 3 * DM * 2) + 32 * 3 * DM * 2, d);
        if (j == 4) glds16(dm.vm0, dm.mg + (size_t)s * (64 * FF * 2), d);
        if (j == 5 && WM == 0) glds16(dm.vm1, dm.mg + (size_t)s * (64 * FF * 2), d);
    };
    auto rd = [&](auto F_, int n, unsigned so) {
        constexpr int F = decltype(F_)::v;
        const int k = n == 0 ? 0 : n < 3 ? n + 3 : n < 6 ? n - 2 : n;
        if (k < 4) xf[F][k] = lds_rd128(lds + so + xa[F][k >> 1] + (k & 1) * 2048);
        else if (((k - 4) & 1) == 0) wl[F][(k - 4) >> 1] = tr_read(lds + so + wa[F][(k - 4) >> 1]);
        else wh[F][(k - 4) >> 1] = tr_read(lds + so + wa[F][(k - 4) >> 1] + HI);
    };
    auto mm = [&](auto F_, int n) {
        constexpr int F = decltype(F_)::v;
        if (n < 4 * NT) {
            const int i = n >> 2, m = n & 3;
            const u32x4 wf = {wl[F][i][0], wl[F][i][1], wh[F][i][0], wh[F][i][1]};
            acc[m][i] = TYPE == 1 ? mfma16(xf[F][m], wf, acc[m][i]) : mfma16(wf, xf[F][m], acc[m][i]);
        } else if (TYPE == 2 && n < 16) {
            const int m = n & 1;
            if (n < 14) st[m] = mfma16(ones, xf[F][m], st[m]);
            else st[2 + m] = mfma16(xf[F][m], xf[F][m], st[2 + m]);
        } else {
            const int i = n - 16;
            const u32x4 wf = {wl[F][i][0], wl[F][i][1], wh[F][i][0], wh[F][i][1]};
            cs[i] = TYPE == 1 ? mfma16(ones, wf, cs[i]) : mfma16(wf, ones, cs[i]);
        }
    };
    auto phase = [&](auto F_, auto RD_, auto DMA_, unsigned rso, int ds, unsigned dso) {
        constexpr int F = decltype(F_)::v;
        constexpr int NM = F == 0 ? 16 + NT : 16;
#pragma unroll
        for (int n = 0; n < NM; ++n) {
            mm(F_, n);
            if (decltype(RD_)::v && n < NR) rd(IC<1 - F>{}, n, rso);
            SBAR();
            if (decltype(DMA_)::v && n % 3 == 0 && n / 3 < 6) { piece(n / 3, ds, dso); SBAR(); }
        }
    };
#pragma unroll
    for (int s = 0; s < 3; ++s)
#pragma unroll
        for (int j = 0; j < 6; ++j) piece(j, s, s * K1_STAGE);
    glds16(dm.tv, dm.tsrc0, dm.dst + K1_TR);
    glds16(dm.tv, dm.tsrc1, dm.dst + K1_TR + 8192);
    if (WM == 0) { WAIT_BAR(14); } else { WAIT_BAR(12); }
    STAMP(lds, K1_LDS, 1);
#pragma unroll
    for (int n = 0; n < NR; ++n) rd(IC<0>{}, n, 0);
    phase(IC<0>{}, IC<1>{}, IC<0>{}, 0, 0, 0);
    if (WM == 0) { WAIT_BAR(8); } else { WAIT_BAR(7); }
    unsigned so = 0;
    for (int s = 0; s < 7; ++s) {
        STAMP(lds, K1_LDS, 2 + s);
        const unsigned sn = so == 2 * K1_STAGE ? 0 : so + K1_STAGE;
        phase(IC<1>{}, IC<1>{}, IC<WM == 0>{}, sn, s + 3, so);
        phase(IC<0>{}, IC<1>{}, IC<WM == 1>{}, sn, s + 3, so);
        if (WM == 0) { WAIT_BAR(6); } else { WAIT_BAR(5); }
        so = sn;
    }
    for (int s = 7; s < 9; ++s) {
        STAMP(lds, K1_LDS, 2 + s);
        const unsigned sn = so == 2 * K1_STAGE ? 0 : so + K1_STAGE;
        phase(IC<1>{}, IC<1>{}, IC<0>{}, sn, 0, 0);
        phase(IC<0>{}, IC<1>{}, IC<0>{}, sn, 0, 0);
        if (s == 7) { WAIT_BAR(0); }
        so = sn;
    }
    STAMP(lds, K1_LDS, 11);
    phase(IC<1>{}, IC<0>{}, IC<0>{}, 0, 0, 0);
}

template <int TYPE>
__device__ __forceinline__ void k1_body(lds_ptr lds, const K1Dma& dm, int wm, int wn, int T0, const u16* x, const u16* b1, char* ws,
                                        int mb, int lane, bool live) {
    constexpr int NT = TYPE == 2 ? 3 : 4;
    const int g = lane >> 4, l15 = lane & 15;
    const int pm = TYPE == 2 ? 2 * (wn & 1) : 0;
    u32x2 bb[3];
    if constexpr (TYPE == 2) {
#pragma unroll
        for (int i = 0; i < 3; ++i) {
            const u16* bp = b1 + (T0 + i) * 16 + g * 4;
            asm volatile("global_load_dwordx2 %0, %1, off" : "=v"(bb[i]) : "v"(bp) : "memory");
        }
    }
    unsigned xa[2][2], wa[2][4];
    {
        const int t = wm * 64 + l15, f = (t >> 1) & 7;
        const int q = (lane >> 2) & 3, p = lane & 3;
#pragma unroll
        for (int F = 0; F < 2; ++F) {
            const int ks = wm ^ F;
            const unsigned xb = t * 128 + (((4 * ks + g) ^ f) * 16);
            xa[F][0] = xb + pm * 2048;
            xa[F][1] = xb + (2 - pm) * 2048;
#pragma unroll
            for (int a = 0; a < 4; ++a) {
                if (TYPE == 2) wa[F][a] = K1_MOFF + (ks * 4 + g) * 1536 + q * 192 + (((3 * (wn & 1) + a) ^ (g & 1)) * 32) + p * 8;
                else wa[F][a] = K1_QOFF + (ks * 4 + g) * 2048 + wn * 1024 + q * 128 + ((a ^ (((q >> 1) & 1) | ((g & 1) << 1))) * 32) + p * 8;
            }
        }
    }
    f32x4 acc[4][4], cs[4], st[4];
#pragma unroll
    for (int i = 0; i < 4; ++i) {
        cs[i] = f32x4{0.f, 0.f, 0.f, 0.f};
        st[i] = f32x4{0.f, 0.f, 0.f, 0.f};
#pragma unroll
        for (int m = 0; m < 4; ++m) acc[m][i] = f32x4{0.f, 0.f, 0.f, 0.f};
    }
    if (wm == 0) k1_loop<TYPE, 0>(lds, dm, xa, wa, acc, cs, st);
    else k1_loop<TYPE, 1>(lds, dm, xa, wa, acc, cs, st);
    STAMP(lds, K1_LDS, 12);
    if constexpr (TYPE == 2) {
        if ((l15 >> 2) == g) {
#pragma unroll
            for (int m = 0; m < 2; ++m) {
                const int r = l15 & 3;
                const float rq = r == 0 ? st[2 + m][0] : r == 1 ? st[2 + m][1] : r == 2 ? st[2 + m][2] : st[2 + m][3];
                const int tl = wm * 64 + (pm + m) * 16 + l15;
                const float mu = st[m][0] * (1.0f / DM);
                float var = __builtin_fmaf(-mu, mu, rq * (1.0f / DM));
                if (!(var >= mu * mu * (1.0f / 256.0f))) {
                    const u16* xr = x + (size_t)(mb * 128 + tl) * DM;
                    float a = 0.f;
                    for (int k = 0; k < DM; ++k) {
                        float d = __uint_as_float((unsigned)xr[k] << 16) - mu;
                        a = __builtin_fmaf(d, d, a);
                    }
                    var = a * (1.0f / DM);
                }
                const float r1 = __builtin_amdgcn_rsqf(var + 1e-5f);
                const float r2 = r1 * __builtin_amdgcn_rsqf(__builtin_fmaf(var * r1, r1, 1e-5f));
                lds_wrf2(lds + K1_ROWST + tl * 8, f32x2{-mu * r1, r1});
                lds_wrf2(lds + K1_ROWST + 1024 + tl * 8, f32x2{-mu * r2, r2});
            }
        }
    }
    {
        lds_ptr cp = lds + K1_COLS + (wm * 4 + wn) * 256;
#pragma unroll
        for (int i = 0; i < NT; ++i) {
            if (TYPE == 1) { if (g == 0) lds_wrf(cp + i * 64 + l15 * 4, cs[i][0]); }
            else if (l15 == 0) lds_wrf4(cp + i * 64 + g * 16, cs[i]);
        }
    }
    LGKM_BAR();
    STAMP(lds, K1_LDS, 13);
    const int tok0 = mb * 128 + wm * 64;
    lds_ptr cp = lds + K1_COLS + ((1 - wm) * 4 + wn) * 256;
    if constexpr (TYPE == 1) {
        float c[4];
#pragma unroll
        for (int i = 0; i < 4; ++i) c[i] = cs[i][0] + lds_rdf(cp + i * 64 + l15 * 4);
#pragma unroll
        for (int mp = 0; mp < 2; ++mp) {
            f32x4 sa[2][2];
#pragma unroll
            for (int m = 0; m < 2; ++m) {
                sa[m][0] = lds_rdf4(lds + K1_ROWST + (wm * 64 + (2 * mp + m) * 16 + g * 4) * 8);
                sa[m][1] = lds_rdf4(lds + K1_ROWST + (wm * 64 + (2 * mp + m) * 16 + g * 4) * 8 + 16);
            }
            const int to = (tok0 >> 3) + mp * 4 + ((g & 1) * 2 + (g >> 1));
            char* vp = ws + WS_V + ((size_t)to * DM + (T0 - 80) * 16 + l15) * 16;
#pragma unroll
            for (int i = 0; i < 4; ++i) {
                unsigned pkd[2][2];
#pragma unroll
                for (int m = 0; m < 2; ++m) {
                    float v[4];
#pragma unroll
                    for (int r = 0; r < 4; ++r)
                        v[r] = __builtin_fmaf(acc[2 * mp + m][i][r], sa[m][r >> 1][(r & 1) * 2 + 1], sa[m][r >> 1][(r & 1) * 2] * c[i]);
                    pkd[m][0] = pk2(v[0], v[1]);
                    pkd[m][1] = pk2(v[2], v[3]);
                }
                auto s0 = __builtin_amdgcn_permlane16_swap(pkd[0][0], pkd[1][0], false, false);
                auto s1 = __builtin_amdgcn_permlane16_swap(pkd[0][1], pkd[1][1], false, false);
                if (live) *(u32x4*)(vp + i * 256) = u32x4{s0[0], s1[0], s0[1], s1[1]};
            }
        }
    } else {
        f32x2 sr[4];
        const float sc = (TYPE == 0 && T0 < 40) ? 0.22360679774997896f * 1.4426950408889634f : 1.0f;
#pragma unroll
        for (int m = 0; m < 4; ++m) {
            sr[m] = lds_rdf2(lds + K1_ROWST + (TYPE == 2 ? 1024 : 0) + (wm * 64 + (m ^ pm) * 16 + l15) * 8);
            if (TYPE == 0) sr[m] *= sc;
        }
        if constexpr (TYPE == 2) { PIN(bb[0]); PIN(bb[1]); PIN(bb[2]); }
        char* op = ws + (TYPE == 2 ? WS_H : WS_QK) + ((size_t)(2 * T0 + (g >> 1)) * SEQ + tok0 + (g & 1) * 16 + l15) * 16;
#pragma unroll
        for (int i = 0; i < NT; ++i) {
            const f32x4 c = cs[i] + lds_rdf4(cp + i * 64 + g * 16);
            const f32x2 c01 = {c[0], c[1]}, c23 = {c[2], c[3]};
            f32x2 b01 = {0.f, 0.f}, b23 = {0.f, 0.f};
            if constexpr (TYPE == 2) {
                b01 = f32x2{bf_lo(bb[i][0]), bf_hi(bb[i][0])};
                b23 = f32x2{bf_lo(bb[i][1]), bf_hi(bb[i][1])};
            }
#pragma unroll
            for (int mp = 0; mp < 2; ++mp) {
                unsigned pkd[2][2];
#pragma unroll
                for (int m = 0; m < 2; ++m) {
                    const f32x4 a = acc[2 * mp + m][i];
                    const f32x2 nm = {sr[2 * mp + m][0], sr[2 * mp + m][0]}, rr = {sr[2 * mp + m][1], sr[2 * mp + m][1]};
                    f32x2 u0, u1;
                    if constexpr (TYPE == 2) {
                        u0 = gelu_tanh2(__builtin_elementwise_fma(f32x2{a[0], a[1]}, rr, __builtin_elementwise_fma(nm, c01, b01)));
                        u1 = gelu_tanh2(__builtin_elementwise_fma(f32x2{a[2], a[3]}, rr, __builtin_elementwise_fma(nm, c23, b23)));
                    } else {
                        u0 = __builtin_elementwise_fma(f32x2{a[0], a[1]}, rr, nm * c01);
                        u1 = __builtin_elementwise_fma(f32x2{a[2], a[3]}, rr, nm * c23);
                    }
                    pkd[m][0] = pk2(u0[0], u0[1]);
                    pkd[m][1] = pk2(u1[0], u1[1]);
                }
                auto s0 = __builtin_amdgcn_permlane16_swap(pkd[0][0], pkd[1][0], false, false);
                auto s1 = __builtin_amdgcn_permlane16_swap(pkd[0][1], pkd[1][1], false, false);
                *(u32x4*)(op + (size_t)i * 2 * SEQ * 16 + ((2 * mp) ^ pm) * 256) = u32x4{s0[0], s1[0], s0[1], s1[1]};
            }
        }
    }
}

__global__ __launch_bounds__(512, 2) void k1_qkv_mlp1(const u16* __restrict__ x, const u16* __restrict__ wqkv, const u16* __restrict__ w1,
                                                      const u16* __restrict__ b1, const u16* __restrict__ wo, const u16* __restrict__ w2,
                                                      char* __restrict__ ws) {
    __shared__ __attribute__((aligned(1024))) char smem[K1_LDS + PROF_LDS];
    lds_ptr lds = (lds_ptr)smem;
    const unsigned lds0 = (unsigned)(uintptr_t)lds;
    const int lane = threadIdx.x & 63;
    const int w = __builtin_amdgcn_readfirstlane(threadIdx.x >> 6);
    const int b = blockIdx.x, xcd = b & 7, idx = b >> 3;
    const int mb = (xcd & 1) * 8 + (idx & 7), cg = (xcd >> 1) * 4 + (idx >> 3);
    const int cq = cg < 15 ? cg : 14;
    STAMP(lds, K1_LDS, 0);
    K1Dma dm;
    {
        const int q8 = lane >> 3, c = lane & 7;
        const int r = 8 * w + q8;
        dm.vx = r * (DM * 2) + ((c ^ ((r >> 1) & 7)) * 16);
        const int ko = w >> 1;
        const int a = (c >> 1) ^ (((q8 >> 1) & 1) | ((ko & 1) << 1));
        dm.vq = (8 * ko + q8) * (3 * DM * 2) + (w & 1) * 128 + a * 32 + (c & 1) * 16;
        unsigned vm[2];
#pragma unroll
        for (int i = 0; i < 2; ++i) {
            const int u = ((w + 8 * i) * 64 + lane) % 768;
            const int k2 = u / 96, rem = u - k2 * 96, row = rem / 12, c2 = rem - row * 12;
            vm[i] = (8 * k2 + row) * (FF * 2) + (((c2 >> 1) ^ (k2 & 1)) * 32) + (c2 & 1) * 16;
        }
        dm.vm0 = vm[0];
        dm.vm1 = vm[1];
        dm.xg = (const char*)x + (size_t)mb * 128 * DM * 2;
        dm.qg = (const char*)wqkv + cq * 256;
        dm.mg = (const char*)w1 + cg * 192;
        dm.dst = lds0 + w * 1024;
    }
    const int pid0 = b + 256 * w, pid1 = pid0 + 2048;
    const bool t1 = pid1 < 2720;
    const int pl0 = pid0 / 10, cb0 = pid0 - pl0 * 10, pl1 = t1 ? pid1 / 10 : 0, cb1 = t1 ? pid1 - pl1 * 10 : 0;
    dm.tv = (lane >> 3) * (DM * 2) + (lane & 7) * 16;
    dm.tsrc0 = (pl0 < 80 ? (const char*)wo + (size_t)pl0 * 8 * DM * 2 : (const char*)w2 + (size_t)(pl0 - 80) * 8 * DM * 2) + cb0 * 128;
    dm.tsrc1 = (const char*)w2 + (size_t)(t1 ? pl1 - 80 : 0) * 8 * DM * 2 + cb1 * 128;

    const int wm = w >> 2, wn = (w & 3) ^ (wm << 1);
    if (wn >= 2) k1_body<2>(lds, dm, wm, wn, 3 * (2 * cg + wn - 2), x, b1, ws, mb, lane, true);
    else if (cg >= 10) k1_body<1>(lds, dm, wm, wn, 4 * (2 * cq + wn), x, b1, ws, mb, lane, cg < 15);
    else k1_body<0>(lds, dm, wm, wn, 4 * (2 * cq + wn), x, b1, ws, mb, lane, true);
    {
        const int g = lane >> 4, q = (lane >> 2) & 3, p = lane & 3;
        lds_ptr tp = lds + K1_TR + w * 1024 + q * 128 + g * 32 + p * 8;
        u32x2 lo = tr_read(tp), hi = tr_read(tp + 512);
        *(u32x4*)(ws + WS_WT + ((size_t)pl0 * DM + cb0 * 64 + lane) * 16) = u32x4{lo[0], lo[1], hi[0], hi[1]};
        if (t1) {
            lo = tr_read(tp + 8192);
            hi = tr_read(tp + 8192 + 512);
            *(u32x4*)(ws + WS_WT + ((size_t)pl1 * DM + cb1 * 64 + lane) * 16) = u32x4{lo[0], lo[1], hi[0], hi[1]};
        }
    }
}

constexpr int K2_SLOT = 22528, K2_VOFF = 12288, K2_NSLOT = 6;
constexpr int K2_ONES = K2_NSLOT * K2_SLOT;
constexpr int K2_FLAG = K2_ONES + 10240;
constexpr int K2_LDS = K2_FLAG + 64;

template <int MODE>
__device__ __forceinline__ void k2_pass(lds_ptr lds, unsigned lds0, const char* ws, int w, const unsigned* doff, const unsigned* dstr,
                                        unsigned kmain0, unsigned kx0, unsigned vb0, unsigned vinc, u32x4 q0, u32x4 q1, bool first,
                                        float& mref, f32x16& o) {
    auto issue = [&](int c) {
        const int slot = c >= K2_NSLOT ? c - K2_NSLOT : c;
        unsigned d = lds0 + slot * K2_SLOT + w * 1024;
        glds16(doff[0], ws + (size_t)c * dstr[0], d);
        glds16(doff[1], ws + (size_t)c * dstr[1], d + 8192);
        if (w < 6) glds16(doff[2], ws + (size_t)c * dstr[2], d + 16384);
    };
#pragma unroll
    for (int c = 0; c < K2_NSLOT; ++c) issue(c);
    if (w < 6) { WAIT_VM(15); } else { WAIT_VM(10); }
    LGKM_BAR();
    asm volatile("" : "+v"(q0), "+v"(q1));
    unsigned km = kmain0, kx = kx0, vb = vb0;
    u32x4 kf0 = lds_rd128(lds + km), kf1 = lds_rd128(lds + kx);
    f32x16 negm;
    f32x16 zero16;
#pragma unroll
    for (int r = 0; r < 16; ++r) zero16[r] = 0.f;
    if constexpr (MODE == 0) {
        if (first) {
            f32x16 s = mfma32(kf0, q0, zero16);
            s = mfma32(kf1, q1, s);
            float m = s[0];
#pragma unroll
            for (int r = 1; r < 16; ++r) m = __builtin_fmaxf(m, s[r]);
            auto rr = __builtin_amdgcn_permlane32_swap(__float_as_uint(m), __float_as_uint(m), false, false);
            mref = __builtin_fmaxf(__uint_as_float(rr[0]), __uint_as_float(rr[1]));
        }
#pragma unroll
        for (int r = 0; r < 16; ++r) negm[r] = -mref;
    } else {
        negm = zero16;
    }
    PIN(negm);
    float mx = -__builtin_inff();
    f32x16 sc = mfma32(kf0, q0, negm);
    sc = mfma32(kf1, q1, sc);
    kf0 = lds_rd128(lds + km + 512);
    kf1 = lds_rd128(lds + kx + 512);
    o = zero16;
    for (int c = 0; c < 8; ++c) {
        const unsigned step = (c == K2_NSLOT - 1) ? (unsigned)(-(K2_NSLOT - 1) * K2_SLOT) : (unsigned)K2_SLOT;
        const unsigned kmn = km + step, kxn = kx + step;
#pragma unroll
        for (int tt = 0; tt < 8; ++tt) {
            if (tt == 6) {
                if (c == 0 || c == 5) { WAIT_BAR(0); }
                else if (c == 2) { LGKM_BAR(); issue(6); issue(7); }
            }
            f32x16 sn = mfma32(kf0, q0, negm);
            sn = mfma32(kf1, q1, sn);
            if (tt < 6) {
                kf0 = lds_rd128(lds + km + (tt + 2) * 512);
                kf1 = lds_rd128(lds + kx + (tt + 2) * 512);
            } else {
                kf0 = lds_rd128(lds + kmn + (tt - 6) * 512);
                kf1 = lds_rd128(lds + kxn + (tt - 6) * 512);
            }
            if constexpr (MODE == 0) {
                u32x4 vf0 = lds_rd128(lds + vb + tt * 1280);
                u32x4 vf1 = lds_rd128(lds + vb + tt * 1280 + 640);
                u32x4 p0, p1;
#pragma unroll
                for (int r = 0; r < 4; ++r) p0[r] = pk2(__builtin_amdgcn_exp2f(sc[2 * r]), __builtin_amdgcn_exp2f(sc[2 * r + 1]));
                o = mfma32(vf0, p0, o);
#pragma unroll
                for (int r = 0; r < 4; ++r) p1[r] = pk2(__builtin_amdgcn_exp2f(sc[8 + 2 * r]), __builtin_amdgcn_exp2f(sc[9 + 2 * r]));
                o = mfma32(vf1, p1, o);
            } else {
#pragma unroll
                for (int r = 0; r < 16; ++r) mx = __builtin_fmaxf(mx, sc[r]);
            }
            sc = sn;
        }
        km = kmn;
        kx = kxn;
        vb += (c == K2_NSLOT - 1) ? (unsigned)(-(K2_NSLOT - 1)) * vinc : vinc;
    }
    if constexpr (MODE == 1) {
        auto rr = __builtin_amdgcn_permlane32_swap(__float_as_uint(mx), __float_as_uint(mx), false, false);
        mref = __builtin_fmaxf(__uint_as_float(rr[0]), __uint_as_float(rr[1]));
    }
}

__global__ __launch_bounds__(512, 2) void k2_attn(char* __restrict__ ws) {
    __shared__ __attribute__((aligned(1024))) char smem[K2_LDS];
    lds_ptr lds = (lds_ptr)smem;
    const unsigned lds0 = (unsigned)(uintptr_t)lds;
    const int tid = threadIdx.x, lane = tid & 63, r32 = lane & 31, hi = lane >> 5;
    const int w = __builtin_amdgcn_readfirstlane(tid >> 6);
    const int b = blockIdx.x, xcd = b & 7, idx = b >> 3;
    const int h = xcd * 4 + (idx >> 3), qb = idx & 7;
    const int odd = h & 1, plane0 = 5 * (h >> 1) + 2 * odd;
    const int mp = odd, xp = odd ? 0 : 2;
    const int tok = qb * 256 + w * 32 + r32;
    u32x4 q0, q1;
    {
        const char* qp0 = ws + WS_QK + ((size_t)(plane0 + mp + hi) * SEQ + tok) * 16;
        const char* qp1 = ws + WS_QK + ((size_t)(plane0 + xp) * SEQ + tok) * 16;
        asm volatile("global_load_dwordx4 %0, %1, off" : "=v"(q0) : "v"(qp0) : "memory");
        asm volatile("global_load_dwordx4 %0, %1, off" : "=v"(q1) : "v"(qp1) : "memory");
    }
    for (int i = tid; i < 10240 / 16; i += 512) *(__attribute__((address_space(3))) u32x4*)(lds + K2_ONES + i * 16) = u32x4{ONES2, ONES2, ONES2, ONES2};
    if (tid == 0) *(__attribute__((address_space(3))) unsigned*)(lds + K2_FLAG) = 0u;
    unsigned doff[3], dstr[3];
#pragma unroll
    for (int i = 0; i < 3; ++i) {
        const int n = w + 8 * i;
        if (n < 12) {
            doff[i] = (unsigned)WS_QK + ((80 + plane0 + (n >> 2)) * SEQ + (n & 3) * 64 + lane) * 16;
            dstr[i] = 256 * 16;
        } else {
            const int u = (n - 12) * 64 + lane, to = u / 20, d = u - to * 20;
            doff[i] = (unsigned)WS_V + (to * DM + 20 * h + d) * 16;
            dstr[i] = 32 * DM * 16;
        }
    }
    const int kap = (r32 & 19) | ((r32 & 4) << 1) | ((r32 & 8) >> 1);
    const unsigned kmain0 = (mp + hi) * 4096 + kap * 16, kx0 = xp * 4096 + kap * 16;
    unsigned vb0, vinc;
    if (r32 >= 20 && r32 < 28) { vb0 = K2_ONES + hi * 320 + 128; vinc = 0; }
    else { vb0 = K2_VOFF + hi * 320 + (r32 < 20 ? r32 : 16) * 16; vinc = K2_SLOT; }
    WAIT_VM(0);
    asm volatile("" : "+v"(q0), "+v"(q1));
    if (hi) q1 = u32x4{0u, 0u, 0u, 0u};
    else if (odd) { q1[0] = 0u; q1[1] = 0u; }
    else { q1[2] = 0u; q1[3] = 0u; }

    float mref = 0.f;
    f32x16 o;
    k2_pass<0>(lds, lds0, ws, w, doff, dstr, kmain0, kx0, vb0, vinc, q0, q1, true, mref, o);
    float l = hi ? o[8] : o[12];
    {
        bool ok = l <= 1.2676506e30f;
#pragma unroll
        for (int r = 0; r < 12; ++r) ok = ok && (__builtin_fabsf(o[r]) < __builtin_inff());
        if (!ok) *(__attribute__((address_space(3))) unsigned*)(lds + K2_FLAG) = 1u;
        LGKM_BAR();
        const unsigned fl = __builtin_amdgcn_readfirstlane(*(__attribute__((address_space(3))) unsigned*)(lds + K2_FLAG));
        if (fl) {
            k2_pass<1>(lds, lds0, ws, w, doff, dstr, kmain0, kx0, vb0, vinc, q0, q1, false, mref, o);
            LGKM_BAR();
            k2_pass<0>(lds, lds0, ws, w, doff, dstr, kmain0, kx0, vb0, vinc, q0, q1, false, mref, o);
            l = hi ? o[8] : o[12];
        }
    }
    const float inv = 1.0f / l;
#pragma unroll
    for (int qd = 0; qd < 3; ++qd) {
        const int col = 20 * h + 8 * qd + 4 * hi;
        u32x2 v = {pk2(o[4 * qd] * inv, o[4 * qd + 1] * inv), pk2(o[4 * qd + 2] * inv, o[4 * qd + 3] * inv)};
        if (qd < 2 || hi == 0) *(u32x2*)(ws + WS_O + ((size_t)(col >> 3) * SEQ + tok) * 16 + ((col >> 2) & 1) * 8) = v;
    }
}

constexpr int K3_LDS = 5 * 16 * 1024;

__global__ __launch_bounds__(320, 2) void k3_out(const u16* __restrict__ x, const u16* __restrict__ b2, const char* __restrict__ ws,
                                                 u16* __restrict__ out) {
    __shared__ __attribute__((aligned(1024))) char smem[K3_LDS];
    lds_ptr lds = (lds_ptr)smem;
    const int tid = threadIdx.x, lane = tid & 63, g = lane >> 4, l15 = lane & 15;
    const int w = __builtin_amdgcn_readfirstlane(tid >> 6);
    const int b = blockIdx.x, xcd = b & 7, idx = b >> 3;
    const int mb = xcd * 4 + (idx & 3), nb = idx >> 2;
    f32x4 acc[4][5];
#pragma unroll
    for (int m = 0; m < 4; ++m)
#pragma unroll
        for (int i = 0; i < 5; ++i) acc[m][i] = f32x4{0.f, 0.f, 0.f, 0.f};
    for (int grp = w; grp < 17; grp += 5) {
        const char* ap = ws + WS_O + ((size_t)(grp * 16 + g) * SEQ + mb * 64 + l15) * 16;
        const char* wp = ws + WS_WT + ((size_t)(grp * 16 + g) * DM + nb * 80 + l15) * 16;
        u32x4 af[4][4], wf[4][5];
#pragma unroll
        for (int ks = 0; ks < 4; ++ks) {
#pragma unroll
            for (int i = 0; i < 5; ++i) wf[ks][i] = *(const u32x4*)(wp + (size_t)ks * 4 * DM * 16 + i * 256);
#pragma unroll
            for (int m = 0; m < 4; ++m) af[ks][m] = *(const u32x4*)(ap + (size_t)ks * 4 * SEQ * 16 + m * 256);
        }
        SBAR();
#pragma unroll
        for (int ks = 0; ks < 4; ++ks)
#pragma unroll
            for (int m = 0; m < 4; ++m)
#pragma unroll
                for (int i = 0; i < 5; ++i) acc[m][i] = mfma16(wf[ks][i], af[ks][m], acc[m][i]);
    }
#pragma unroll
    for (int i = 0; i < 5; ++i)
        if (i != w) {
            const int sl = w - (w > i);
#pragma unroll
            for (int m = 0; m < 4; ++m) lds_wrf4(lds + (((i * 4 + sl) * 4 + m) * 64 + lane) * 16, acc[m][i]);
        }
    f32x4 own[4];
#pragma unroll
    for (int m = 0; m < 4; ++m) own[m] = w == 0 ? acc[m][0] : w == 1 ? acc[m][1] : w == 2 ? acc[m][2] : w == 3 ? acc[m][3] : acc[m][4];
    const int col = nb * 80 + w * 16 + g * 4;
    u32x2 xv[4];
#pragma unroll
    for (int m = 0; m < 4; ++m) xv[m] = *(const u32x2*)(x + (size_t)(mb * 64 + m * 16 + l15) * DM + col);
    const u32x2 bv = *(const u32x2*)(b2 + col);
    LGKM_BAR();
#pragma unroll
    for (int m = 0; m < 4; ++m) {
        lds_ptr p = lds + ((w * 16 + m) * 64 + lane) * 16;
        f32x4 s = own[m] + ((lds_rdf4(p) + lds_rdf4(p + 4096)) + (lds_rdf4(p + 8192) + lds_rdf4(p + 12288)));
        u32x2 r;
        r[0] = pk2(s[0] + bf_lo(xv[m][0]) + bf_lo(bv[0]), s[1] + bf_hi(xv[m][0]) + bf_hi(bv[0]));
        r[1] = pk2(s[2] + bf_lo(xv[m][1]) + bf_lo(bv[1]), s[3] + bf_hi(xv[m][1]) + bf_hi(bv[1]));
        *(u32x2*)(out + (size_t)(mb * 64 + m * 16 + l15) * DM + col) = r;
    }
}

extern "C" void kernel_launch(void* const* d_in, const int* in_sizes, int n_in, void* d_out, int out_size, void* d_ws, size_t ws_size,
                              hipStream_t stream) {
    const u16* x = (const u16*)d_in[0];
    const u16* wqkv = (const u16*)d_in[1];
    const u16* wo = (const u16*)d_in[2];
    const u16* w1 = (const u16*)d_in[4];
    const u16* b1 = (const u16*)d_in[5];
    const u16* w2 = (const u16*)d_in[6];
    const u16* b2 = (const u16*)d_in[7];
    char* ws = (char*)d_ws;
    k1_qkv_mlp1<<<256, 512, 0, stream>>>(x, wqkv, w1, b1, wo, w2, ws);
    k2_attn<<<256, 512, 0, stream>>>(ws);
    k3_out<<<256, 320, 0, stream>>>(x, b2, ws, (u16*)d_out);
}
```

```cpp
#include <hip/hip_runtime.h>
#include <stdint.h>

typedef __attribute__((ext_vector_type(8))) __bf16 bf16x8;
typedef __attribute__((ext_vector_type(2))) __bf16 bf16x2;
typedef __attribute__((ext_vector_type(4))) float f32x4;
typedef __attribute__((ext_vector_type(2))) float f32x2;
typedef __attribute__((ext_vector_type(16))) float f32x16;
typedef __attribute__((ext_vector_type(4))) unsigned u32x4;
typedef __attribute__((ext_vector_type(2))) unsigned u32x2;
typedef short v4i16_t __attribute__((ext_vector_type(4)));
typedef __attribute__((address_space(3))) char* lds_ptr;
typedef unsigned short u16;

#define SBAR() __builtin_amdgcn_sched_barrier(0)
#define PIN(x) asm volatile("" : "+v"(x))
#define WAIT_BAR(N) asm volatile("s_waitcnt vmcnt(" #N ") lgkmcnt(0)\n\ts_barrier" ::: "memory")
#define WAIT_VM(N) asm volatile("s_waitcnt vmcnt(" #N ")" ::: "memory")
#define LGKM_BAR() asm volatile("s_waitcnt lgkmcnt(0)\n\ts_barrier" ::: "memory")

#define PROF_LDS 0
#define STAMP(lds, base, i) do {} while (0)
#define STAMP_DUMP(ws, lds, base, kid, n) do {} while (0)
constexpr int SEQ = 2048, DM = 640, FF = 1536;
constexpr size_t WS_QK = 0;
constexpr size_t WS_V = WS_QK + (size_t)160 * SEQ * 16;
constexpr size_t WS_H = WS_V + (size_t)256 * DM * 16;
constexpr size_t WS_O = WS_H + (size_t)192 * SEQ * 16;

__device__ __forceinline__ void glds16(unsigned voff, const void* sbase, unsigned lds_base) {
    unsigned sv;
    asm volatile("s_mov_b32 %0, m0\n\ts_mov_b32 m0, %3\n\ts_nop 0\n\tglobal_load_lds_dwordx4 %1, %2\n\ts_mov_b32 m0, %0"
                 : "=&s"(sv) : "v"(voff), "s"(sbase), "s"(lds_base) : "memory");
}
__device__ __forceinline__ u32x2 tr_read(lds_ptr p) {
    return __builtin_bit_cast(u32x2, __builtin_amdgcn_ds_read_tr16_b64_v4i16((__attribute__((address_space(3))) v4i16_t*)p));
}
__device__ __forceinline__ u32x4 lds_rd128(lds_ptr p) { return *(__attribute__((address_space(3))) u32x4*)p; }
__device__ __forceinline__ f32x4 lds_rdf4(lds_ptr p) { return *(__attribute__((address_space(3))) f32x4*)p; }
__device__ __forceinline__ f32x4 mfma16(u32x4 a, u32x4 b, f32x4 c) {
    return __builtin_amdgcn_mfma_f32_16x16x32_bf16(__builtin_bit_cast(bf16x8, a), __builtin_bit_cast(bf16x8, b), c, 0, 0, 0);
}
__device__ __forceinline__ f32x16 mfma32(u32x4 a, u32x4 b, f32x16 c) {
    return __builtin_amdgcn_mfma_f32_32x32x16_bf16(__builtin_bit_cast(bf16x8, a), __builtin_bit_cast(bf16x8, b), c, 0, 0, 0);
}
__device__ __forceinline__ float dot2(unsigned a, unsigned b, float c) {
    return __builtin_amdgcn_fdot2_f32_bf16(__builtin_bit_cast(bf16x2, a), __builtin_bit_cast(bf16x2, b), c, false);
}
__device__ __forceinline__ unsigned pk2(float lo, float hi) {
    bf16x2 v = {(__bf16)lo, (__bf16)hi};
    return __builtin_bit_cast(unsigned, v);
}
__device__ __forceinline__ float bf_lo(unsigned u) { return __uint_as_float(u << 16); }
__device__ __forceinline__ float bf_hi(unsigned u) { return __uint_as_float(u & 0xffff0000u); }
__device__ __forceinline__ float sum_groups(float v) {
    auto a = __builtin_amdgcn_permlane16_swap(__float_as_uint(v), __float_as_uint(v), false, false);
    v = __uint_as_float(a[0]) + __uint_as_float(a[1]);
    auto b = __builtin_amdgcn_permlane32_swap(__float_as_uint(v), __float_as_uint(v), false, false);
    return __uint_as_float(b[0]) + __uint_as_float(b[1]);
}
constexpr unsigned ONES2 = 0x3f803f80u;

constexpr int K1_STAGE = 49152, K1_WOFF = 16384;
constexpr int K1_ROWST = 3 * K1_STAGE;
constexpr int K1_COLS = K1_ROWST + 1024;
constexpr int K1_LDS = K1_COLS + 4096;

__device__ __forceinline__ float gelu_tanh(float u) {
    const float c1 = -2.0f * 0.7978845608028654f * 1.4426950408889634f;
    const float c2 = c1 * 0.044715f;
    float t = u * __builtin_fmaf(u * u, c2, c1);
    float e = __builtin_amdgcn_exp2f(t);
    return u * __builtin_amdgcn_rcpf(1.0f + e);
}

template <int NT, int WN, bool ISV, bool MLP>
__device__ __forceinline__ void k1_body(lds_ptr lds, unsigned lds0, const char* xg, const char* wg, int tA, int tB,
                                        const u16* x, const u16* b1, char* ws, int mb, int w, int lane) {
    constexpr int NSUB = MLP ? 3 : 4;
    constexpr int PITCH = MLP ? FF * 2 : 3 * DM * 2;
    const int wm = w & 3;
    const int g = lane >> 4, l15 = lane & 15;
    unsigned xoff0, xoff1, woff;
    {
        int r0 = 16 * w + (lane >> 3), r1 = r0 + 8;
        xoff0 = r0 * (DM * 2) + (((lane & 7) ^ ((r0 >> 1) & 7)) * 16);
        xoff1 = r1 * (DM * 2) + (((lane & 7) ^ ((r1 >> 1) & 7)) * 16);
        int q8 = lane >> 3, pos = lane & 7;
        int a = (pos >> 1) ^ (((q8 >> 1) & 1) | ((w & 1) << 1));
        woff = (8 * w + q8) * PITCH + a * 32 + (pos & 1) * 16;
    }
    auto issue = [&](int s, int slot) {
        const char* xs = xg + s * 128;
        const char* wsrc = wg + (size_t)s * 64 * PITCH;
        unsigned d = lds0 + slot * K1_STAGE;
        glds16(xoff0, xs, d + (2 * w) * 1024);
        glds16(xoff1, xs, d + (2 * w + 1) * 1024);
        unsigned dw = d + K1_WOFF + w * NSUB * 1024;
        if constexpr (MLP) {
            glds16(woff, wsrc + tA * 32, dw);
            glds16(woff, wsrc + tA * 32 + 128, dw + 1024);
            glds16(woff, wsrc + tA * 32 + 256, dw + 2048);
        } else {
            glds16(woff, wsrc + tA * 32, dw);
            glds16(woff, wsrc + tA * 32 + 128, dw + 1024);
            glds16(woff, wsrc + tB * 32, dw + 2048);
            glds16(woff, wsrc + tB * 32 + 128, dw + 3072);
        }
    };
    unsigned xb[2], wb[4];
    {
        int t = wm * 32 + l15;
        int f = (t >> 1) & 7;
        xb[0] = t * 128 + ((g ^ f) * 16);
        xb[1] = t * 128 + (((4 + g) ^ f) * 16);
        int q = (lane >> 2) & 3, p = lane & 3;
        int phi = ((q >> 1) & 1) | ((g & 1) << 1);
#pragma unroll
        for (int a = 0; a < 4; ++a) wb[a] = K1_WOFF + g * NSUB * 1024 + q * 128 + ((a ^ phi) * 32) + p * 8;
    }
    f32x4 acc[2][NT];
#pragma unroll
    for (int m = 0; m < 2; ++m)
#pragma unroll
        for (int i = 0; i < NT; ++i) acc[m][i] = f32x4{0.f, 0.f, 0.f, 0.f};
    float csum[NT];
#pragma unroll
    for (int i = 0; i < NT; ++i) csum[i] = 0.f;
    float rs = 0.f, rq = 0.f;
    u32x4 xf0[2], wf0[NT], xf1[2], wf1[NT];

    auto rd = [&](int slot, int ks, u32x4* xf, u32x4* wf) {
        lds_ptr sp = lds + slot * K1_STAGE;
        xf[0] = lds_rd128(sp + xb[ks]);
        xf[1] = lds_rd128(sp + xb[ks] + 2048);
#pragma unroll
        for (int i = 0; i < NT; ++i) {
            const int tl = MLP ? 6 * WN + i : i;
            const int sub = MLP ? (tl >> 2) : (WN * 2 + (i >> 2));
            lds_ptr p = sp + wb[tl & 3] + ks * 4 * NSUB * 1024 + sub * 1024;
            u32x2 lo = tr_read(p), hi = tr_read(p + 512);
            wf[i] = u32x4{lo[0], lo[1], hi[0], hi[1]};
        }
    };
    auto comp = [&](int kk, const u32x4* xf, const u32x4* wf) {
#pragma unroll
        for (int i = 0; i < NT; ++i) {
            if constexpr (ISV) {
                acc[0][i] = mfma16(xf[0], wf[i], acc[0][i]);
                acc[1][i] = mfma16(xf[1], wf[i], acc[1][i]);
            } else {
                acc[0][i] = mfma16(wf[i], xf[0], acc[0][i]);
                acc[1][i] = mfma16(wf[i], xf[1], acc[1][i]);
            }
        }
#pragma unroll
        for (int e = 0; e < 4; ++e) {
            rs = dot2(xf[WN][e], ONES2, rs);
            rq = dot2(xf[WN][e], xf[WN][e], rq);
        }
        if ((kk & 3) == wm) {
#pragma unroll
            for (int i = 0; i < NT; ++i)
#pragma unroll
                for (int e = 0; e < 4; ++e) csum[i] = dot2(wf[i][e], ONES2, csum[i]);
        }
    };

    STAMP(lds, K1_LDS, 0);
    issue(0, 0);
    issue(1, 1);
    issue(2, 2);
    if constexpr (MLP) { WAIT_BAR(10); } else { WAIT_BAR(12); }
    STAMP(lds, K1_LDS, 1);
    rd(0, 0, xf0, wf0);
    int slot = 0;
    for (int s = 0; s < 10; ++s) {
        rd(slot, 1, xf1, wf1);
        comp(2 * s, xf0, wf0);
        const int nslot = slot == 2 ? 0 : slot + 1;
        if (s < 8) {
            if constexpr (MLP) { WAIT_BAR(5); } else { WAIT_BAR(6); }
        } else {
            WAIT_BAR(0);
        }
        STAMP(lds, K1_LDS, 2 + s);
        if (s < 7) issue(s + 3, slot);
        if (s < 9) rd(nslot, 0, xf0, wf0);
        comp(2 * s + 1, xf1, wf1);
        slot = nslot;
    }
    STAMP(lds, K1_LDS, 12);
    rs = sum_groups(rs);
    rq = sum_groups(rq);
    {
        const int tl = wm * 32 + WN * 16 + l15;
        float mu = rs * (1.0f / DM);
        float var = __builtin_fmaf(-mu, mu, rq * (1.0f / DM));
        if (!(var >= mu * mu * (1.0f / 256.0f))) {
            const u16* xr = x + (size_t)(mb * 128 + tl) * DM;
            float a = 0.f;
            for (int k = 0; k < DM; ++k) {
                float d = __uint_as_float((unsigned)xr[k] << 16) - mu;
                a = __builtin_fmaf(d, d, a);
            }
            var = a * (1.0f / DM);
        }
        float r = 1.0f / __builtin_sqrtf(var + 1e-5f);
        if constexpr (MLP) {
            float v2 = var * r * r;
            r = r / __builtin_sqrtf(v2 + 1e-5f);
        }
        if (g == 0) *(__attribute__((address_space(3))) f32x2*)(lds + K1_ROWST + tl * 8) = f32x2{mu, r};
    }
#pragma unroll
    for (int i = 0; i < NT; ++i) {
        float c = sum_groups(csum[i]);
        if (g == 0) *(__attribute__((address_space(3))) float*)(lds + K1_COLS + wm * 1024 + (WN * 8 + i) * 64 + l15 * 4) = c;
    }
    LGKM_BAR();
    STAMP(lds, K1_LDS, 13);
    const int tok0 = mb * 128 + wm * 32;
    if constexpr (ISV) {
        f32x2 st[2][4];
#pragma unroll
        for (int m = 0; m < 2; ++m) {
            f32x4 a = lds_rdf4(lds + K1_ROWST + (wm * 32 + m * 16 + g * 4) * 8);
            f32x4 b = lds_rdf4(lds + K1_ROWST + (wm * 32 + m * 16 + g * 4) * 8 + 16);
            st[m][0] = f32x2{a[0], a[1]}; st[m][1] = f32x2{a[2], a[3]}; st[m][2] = f32x2{b[0], b[1]}; st[m][3] = f32x2{b[2], b[3]};
        }
        const int to = (tok0 >> 3) + ((g & 1) * 2 + (g >> 1));
        char* vp = ws + WS_V + ((size_t)to * DM + (tA - 80) * 16 + l15) * 16;
#pragma unroll
        for (int i = 0; i < NT; ++i) {
            lds_ptr cp = lds + K1_COLS + (WN * 8 + i) * 64 + l15 * 4;
            typedef __attribute__((address_space(3))) float* lf;
            float cs = (*(lf)cp + *(lf)(cp + 1024)) + (*(lf)(cp + 2048) + *(lf)(cp + 3072));
            unsigned pkd[2][2];
#pragma unroll
            for (int m = 0; m < 2; ++m) {
                float v[4];
#pragma unroll
                for (int r = 0; r < 4; ++r) v[r] = st[m][r][1] * __builtin_fmaf(-st[m][r][0], cs, acc[m][i][r]);
                pkd[m][0] = pk2(v[0], v[1]);
                pkd[m][1] = pk2(v[2], v[3]);
            }
            auto s0 = __builtin_amdgcn_permlane16_swap(pkd[0][0], pkd[1][0], false, false);
            auto s1 = __builtin_amdgcn_permlane16_swap(pkd[0][1], pkd[1][1], false, false);
            *(u32x4*)(vp + i * 256) = u32x4{s0[0], s1[0], s0[1], s1[1]};
        }
    } else {
        f32x2 st[2];
#pragma unroll
        for (int m = 0; m < 2; ++m) st[m] = *(__attribute__((address_space(3))) f32x2*)(lds + K1_ROWST + (wm * 32 + m * 16 + l15) * 8);
        const int T0 = MLP ? tA + 6 * WN : (WN ? tB : tA);
        char* op = ws + (MLP ? WS_H : WS_QK) + ((size_t)(2 * T0 + (g >> 1)) * SEQ + tok0 + (g & 1) * 16 + l15) * 16;
#pragma unroll
        for (int i = 0; i < NT; ++i) {
            lds_ptr cp = lds + K1_COLS + (WN * 8 + i) * 64 + g * 16;
            f32x4 cs = (lds_rdf4(cp) + lds_rdf4(cp + 1024)) + (lds_rdf4(cp + 2048) + lds_rdf4(cp + 3072));
            float bias[4];
            float sc = 1.0f;
            if constexpr (MLP) {
                u32x2 bb = *(const u32x2*)(b1 + (T0 + i) * 16 + g * 4);
                bias[0] = bf_lo(bb[0]); bias[1] = bf_hi(bb[0]); bias[2] = bf_lo(bb[1]); bias[3] = bf_hi(bb[1]);
            } else {
                sc = (T0 + i < 40) ? 0.22360679774997896f * 1.4426950408889634f : 1.0f;
            }
            unsigned pkd[2][2];
#pragma unroll
            for (int m = 0; m < 2; ++m) {
                float v[4];
                const float rr = st[m][1] * sc;
#pragma unroll
                for (int r = 0; r < 4; ++r) {
                    float u = __builtin_fmaf(-st[m][0], cs[r], acc[m][i][r]);
                    if constexpr (MLP) v[r] = gelu_tanh(__builtin_fmaf(u, rr, bias[r]));
                    else v[r] = u * rr;
                }
                pkd[m][0] = pk2(v[0], v[1]);
                pkd[m][1] = pk2(v[2], v[3]);
            }
            auto s0 = __builtin_amdgcn_permlane16_swap(pkd[0][0], pkd[1][0], false, false);
            auto s1 = __builtin_amdgcn_permlane16_swap(pkd[0][1], pkd[1][1], false, false);
            *(u32x4*)(op + (size_t)i * 2 * SEQ * 16) = u32x4{s0[0], s1[0], s0[1], s1[1]};
        }
    }
    WAIT_VM(0); STAMP(lds, K1_LDS, 14); STAMP_DUMP(ws, lds, K1_LDS, 0, 15);
}

__global__ __launch_bounds__(512, 2) void k1_qkv_mlp1(const u16* __restrict__ x, const u16* __restrict__ wqkv, const u16* __restrict__ w1,
                                                      const u16* __restrict__ b1, char* __restrict__ ws) {
    __shared__ __attribute__((aligned(1024))) char smem[K1_LDS + PROF_LDS];
    lds_ptr lds = (lds_ptr)smem;
    const unsigned lds0 = (unsigned)(uintptr_t)lds;
    const int lane = threadIdx.x & 63;
    const int w = __builtin_amdgcn_readfirstlane(threadIdx.x >> 6);
    const int b = blockIdx.x, xcd = b & 7, idx = b >> 3;
    const int mb = (xcd & 1) * 8 + (idx & 7), cb = idx >> 3, j = 2 * (xcd >> 1) + (cb & 1);
    const char* xg = (const char*)x + (size_t)mb * 128 * DM * 2;
    if (cb >= 2) {
        if (w < 4) k1_body<6, 0, false, true>(lds, lds0, xg, (const char*)w1, 12 * j, 0, x, b1, ws, mb, w, lane);
        else k1_body<6, 1, false, true>(lds, lds0, xg, (const char*)w1, 12 * j, 0, x, b1, ws, mb, w, lane);
    } else {
        const int tA = j < 5 ? 80 + 8 * j : 56 + 8 * (j - 5), tB = 7 * j;
        if (w >= 4) k1_body<7, 1, false, false>(lds, lds0, xg, (const char*)wqkv, tA, tB, x, b1, ws, mb, w, lane);
        else if (j < 5) k1_body<8, 0, true, false>(lds, lds0, xg, (const char*)wqkv, tA, tB, x, b1, ws, mb, w, lane);
        else k1_body<8, 0, false, false>(lds, lds0, xg, (const char*)wqkv, tA, tB, x, b1, ws, mb, w, lane);
    }
}

constexpr int K2_SLOT = 22528, K2_VOFF = 12288, K2_NSLOT = 6;
constexpr int K2_ONES = K2_NSLOT * K2_SLOT;
constexpr int K2_FLAG = K2_ONES + 10240;
constexpr int K2_LDS = K2_FLAG + 64;

template <int MODE>
__device__ __forceinline__ void k2_pass(lds_ptr lds, unsigned lds0, const char* ws, int w, const unsigned* doff, const unsigned* dstr,
                                        unsigned kmain0, unsigned kx0, unsigned vb0, unsigned vinc, u32x4 q0, u32x4 q1, bool first,
                                        float& mref, f32x16& o) {
    auto issue = [&](int c) {
        const int slot = c >= K2_NSLOT ? c - K2_NSLOT : c;
        unsigned d = lds0 + slot * K2_SLOT + w * 1024;
        glds16(doff[0], ws + (size_t)c * dstr[0], d);
        glds16(doff[1], ws + (size_t)c * dstr[1], d + 8192);
        if (w < 6) glds16(doff[2], ws + (size_t)c * dstr[2], d + 16384);
    };
#pragma unroll
    for (int c = 0; c < K2_NSLOT; ++c) issue(c);
    if (w < 6) { WAIT_VM(15); } else { WAIT_VM(10); }
    LGKM_BAR();
    asm volatile("" : "+v"(q0), "+v"(q1));
    unsigned km = kmain0, kx = kx0, vb = vb0;
    u32x4 kf0 = lds_rd128(lds + km), kf1 = lds_rd128(lds + kx);
    f32x16 negm;
    f32x16 zero16;
#pragma unroll
    for (int r = 0; r < 16; ++r) zero16[r] = 0.f;
    if constexpr (MODE == 0) {
        if (first) {
            f32x16 s = mfma32(kf0, q0, zero16);
            s = mfma32(kf1, q1, s);
            float m = s[0];
#pragma unroll
            for (int r = 1; r < 16; ++r) m = __builtin_fmaxf(m, s[r]);
            auto rr = __builtin_amdgcn_permlane32_swap(__float_as_uint(m), __float_as_uint(m), false, false);
            mref = __builtin_fmaxf(__uint_as_float(rr[0]), __uint_as_float(rr[1]));
        }
#pragma unroll
        for (int r = 0; r < 16; ++r) negm[r] = -mref;
    } else {
        negm = zero16;
    }
    PIN(negm);
    float mx = -__builtin_inff();
    f32x16 sc = mfma32(kf0, q0, negm);
    sc = mfma32(kf1, q1, sc);
    kf0 = lds_rd128(lds + km + 512);
    kf1 = lds_rd128(lds + kx + 512);
    o = zero16;
    for (int c = 0; c < 8; ++c) {
        const unsigned step = (c == K2_NSLOT - 1) ? (unsigned)(-(K2_NSLOT - 1) * K2_SLOT) : (unsigned)K2_SLOT;
        const unsigned kmn = km + step, kxn = kx + step;
#pragma unroll
        for (int tt = 0; tt < 8; ++tt) {
            if (tt == 6) {
                if (c == 0 || c == 5) { WAIT_BAR(0); }
                else if (c == 2) { LGKM_BAR(); issue(6); issue(7); }
            }
            f32x16 sn = mfma32(kf0, q0, negm);
            sn = mfma32(kf1, q1, sn);
            if (tt < 6) {
                kf0 = lds_rd128(lds + km + (tt + 2) * 512);
                kf1 = lds_rd128(lds + kx + (tt + 2) * 512);
            } else {
                kf0 = lds_rd128(lds + kmn + (tt - 6) * 512);
                kf1 = lds_rd128(lds + kxn + (tt - 6) * 512);
            }
            if constexpr (MODE == 0) {
                u32x4 vf0 = lds_rd128(lds + vb + tt * 1280);
                u32x4 vf1 = lds_rd128(lds + vb + tt * 1280 + 640);
                u32x4 p0, p1;
#pragma unroll
                for (int r = 0; r < 4; ++r) p0[r] = pk2(__builtin_amdgcn_exp2f(sc[2 * r]), __builtin_amdgcn_exp2f(sc[2 * r + 1]));
                o = mfma32(vf0, p0, o);
#pragma unroll
                for (int r = 0; r < 4; ++r) p1[r] = pk2(__builtin_amdgcn_exp2f(sc[8 + 2 * r]), __builtin_amdgcn_exp2f(sc[9 + 2 * r]));
                o = mfma32(vf1, p1, o);
            } else {
#pragma unroll
                for (int r = 0; r < 16; ++r) mx = __builtin_fmaxf(mx, sc[r]);
            }
            sc = sn;
        }
        km = kmn;
        kx = kxn;
        vb += (c == K2_NSLOT - 1) ? (unsigned)(-(K2_NSLOT - 1)) * vinc : vinc;
    }
    if constexpr (MODE == 1) {
        auto rr = __builtin_amdgcn_permlane32_swap(__float_as_uint(mx), __float_as_uint(mx), false, false);
        mref = __builtin_fmaxf(__uint_as_float(rr[0]), __uint_as_float(rr[1]));
    }
}

__global__ __launch_bounds__(512, 2) void k2_attn(char* __restrict__ ws) {
    __shared__ __attribute__((aligned(1024))) char smem[K2_LDS];
    lds_ptr lds = (lds_ptr)smem;
    const unsigned lds0 = (unsigned)(uintptr_t)lds;
    const int tid = threadIdx.x, lane = tid & 63, r32 = lane & 31, hi = lane >> 5;
    const int w = __builtin_amdgcn_readfirstlane(tid >> 6);
    const int b = blockIdx.x, xcd = b & 7, idx = b >> 3;
    const int h = xcd * 4 + (idx >> 3), qb = idx & 7;
    const int odd = h & 1, plane0 = 5 * (h >> 1) + 2 * odd;
    const int mp = odd, xp = odd ? 0 : 2;
    const int tok = qb * 256 + w * 32 + r32;
    u32x4 q0, q1;
    {
        const char* qp0 = ws + WS_QK + ((size_t)(plane0 + mp + hi) * SEQ + tok) * 16;
        const char* qp1 = ws + WS_QK + ((size_t)(plane0 + xp) * SEQ + tok) * 16;
        asm volatile("global_load_dwordx4 %0, %1, off" : "=v"(q0) : "v"(qp0) : "memory");
        asm volatile("global_load_dwordx4 %0, %1, off" : "=v"(q1) : "v"(qp1) : "memory");
    }
    for (int i = tid; i < 10240 / 16; i += 512) *(__attribute__((address_space(3))) u32x4*)(lds + K2_ONES + i * 16) = u32x4{ONES2, ONES2, ONES2, ONES2};
    if (tid == 0) *(__attribute__((address_space(3))) unsigned*)(lds + K2_FLAG) = 0u;
    unsigned doff[3], dstr[3];
#pragma unroll
    for (int i = 0; i < 3; ++i) {
        const int n = w + 8 * i;
        if (n < 12) {
            doff[i] = (unsigned)WS_QK + ((80 + plane0 + (n >> 2)) * SEQ + (n & 3) * 64 + lane) * 16;
            dstr[i] = 256 * 16;
        } else {
            const int u = (n - 12) * 64 + lane, to = u / 20, d = u - to * 20;
            doff[i] = (unsigned)WS_V + (to * DM + 20 * h + d) * 16;
            dstr[i] = 32 * DM * 16;
        }
    }
    const int kap = (r32 & 19) | ((r32 & 4) << 1) | ((r32 & 8) >> 1);
    const unsigned kmain0 = (mp + hi) * 4096 + kap * 16, kx0 = xp * 4096 + kap * 16;
    unsigned vb0, vinc;
    if (r32 >= 20 && r32 < 28) { vb0 = K2_ONES + hi * 320 + 128; vinc = 0; }
    else { vb0 = K2_VOFF + hi * 320 + (r32 < 20 ? r32 : 16) * 16; vinc = K2_SLOT; }
    WAIT_VM(0);
    asm volatile("" : "+v"(q0), "+v"(q1));
    if (hi) q1 = u32x4{0u, 0u, 0u, 0u};
    else if (odd) { q1[0] = 0u; q1[1] = 0u; }
    else { q1[2] = 0u; q1[3] = 0u; }

    float mref = 0.f;
    f32x16 o;
    k2_pass<0>(lds, lds0, ws, w, doff, dstr, kmain0, kx0, vb0, vinc, q0, q1, true, mref, o);
    float l = hi ? o[8] : o[12];
    {
        bool ok = l <= 1.2676506e30f;
#pragma unroll
        for (int r = 0; r < 12; ++r) ok = ok && (__builtin_fabsf(o[r]) < __builtin_inff());
        if (!ok) *(__attribute__((address_space(3))) unsigned*)(lds + K2_FLAG) = 1u;
        LGKM_BAR();
        const unsigned fl = __builtin_amdgcn_readfirstlane(*(__attribute__((address_space(3))) unsigned*)(lds + K2_FLAG));
        if (fl) {
            k2_pass<1>(lds, lds0, ws, w, doff, dstr, kmain0, kx0, vb0, vinc, q0, q1, false, mref, o);
            LGKM_BAR();
            k2_pass<0>(lds, lds0, ws, w, doff, dstr, kmain0, kx0, vb0, vinc, q0, q1, false, mref, o);
            l = hi ? o[8] : o[12];
        }
    }
    const float inv = 1.0f / l;
#pragma unroll
    for (int qd = 0; qd < 3; ++qd) {
        const int col = 20 * h + 8 * qd + 4 * hi;
        u32x2 v = {pk2(o[4 * qd] * inv, o[4 * qd + 1] * inv), pk2(o[4 * qd + 2] * inv, o[4 * qd + 3] * inv)};
        if (qd < 2 || hi == 0) *(u32x2*)(ws + WS_O + ((size_t)(col >> 3) * SEQ + tok) * 16 + ((col >> 2) & 1) * 8) = v;
    }
}

constexpr int K3_LDS = 102400;

__global__ __launch_bounds__(512, 2) void k3_out(const u16* __restrict__ x, const u16* __restrict__ wo, const u16* __restrict__ w2,
                                                 const u16* __restrict__ b2, const char* __restrict__ ws, u16* __restrict__ out) {
    __shared__ __attribute__((aligned(1024))) char smem[K3_LDS];
    lds_ptr lds = (lds_ptr)smem;
    const unsigned lds0 = (unsigned)(uintptr_t)lds;
    const int tid = threadIdx.x, lane = tid & 63, g = lane >> 4, l15 = lane & 15;
    const int w = __builtin_amdgcn_readfirstlane(tid >> 6);
    const int b = blockIdx.x, xcd = b & 7, idx = b >> 3;
    const int mb = xcd * 4 + (idx & 3), nb = idx >> 2;
    const int mh = w & 1, kq = w >> 1;
    f32x4 acc[2][5];
#pragma unroll
    for (int m = 0; m < 2; ++m)
#pragma unroll
        for (int i = 0; i < 5; ++i) acc[m][i] = f32x4{0.f, 0.f, 0.f, 0.f};
    const int q = (lane >> 2) & 3, p = lane & 3;
    const unsigned wb0 = g * 1280 + ((q ^ ((g & 1) << 2)) * 32) + p * 8, wb1 = wb0 ^ 128;
    const int tok0 = mb * 64 + mh * 32;
    for (int ph = 0; ph < 4; ++ph) {
        const int nks = ph == 0 ? 5 : 4;
        const char* wsrc = (ph == 0 ? (const char*)wo : (const char*)w2 + (size_t)(ph - 1) * 512 * DM * 2) + nb * 160;
        const char* asrc = ws + (ph == 0 ? WS_O : WS_H + (size_t)(ph - 1) * 64 * SEQ * 16);
        const int ninstr = nks * 20;
        if (ph) LGKM_BAR();
        for (int n = w; n < ninstr; n += 8) {
            const int u = n * 64 + lane, ko = u / 80, r = u - ko * 80, t = r >> 4, pos = (r >> 1) & 7, hf = r & 1;
            const int row = ko * 8 + (pos ^ ((ko & 1) << 2));
            glds16(row * (DM * 2) + t * 32 + hf * 16, wsrc, lds0 + n * 1024);
        }
        u32x4 af[5][2];
#pragma unroll
        for (int ks = 0; ks < 5; ++ks)
            if (ks < nks) {
#pragma unroll
                for (int m = 0; m < 2; ++m)
                    af[ks][m] = *(const u32x4*)(asrc + ((size_t)((kq * nks + ks) * 4 + g) * SEQ + tok0 + m * 16 + l15) * 16);
            }
        WAIT_BAR(0);
#pragma unroll
        for (int ks = 0; ks < 5; ++ks)
            if (ks < nks) {
                lds_ptr sp = lds + (kq * nks + ks) * 5120;
#pragma unroll
                for (int i = 0; i < 5; ++i) {
                    u32x2 lo = tr_read(sp + wb0 + i * 256), hi2 = tr_read(sp + wb1 + i * 256);
                    u32x4 wf = u32x4{lo[0], lo[1], hi2[0], hi2[1]};
                    acc[0][i] = mfma16(wf, af[ks][0], acc[0][i]);
                    acc[1][i] = mfma16(wf, af[ks][1], acc[1][i]);
                }
            }
    }
    LGKM_BAR();
#pragma unroll
    for (int m = 0; m < 2; ++m)
#pragma unroll
        for (int i = 0; i < 5; ++i)
            *(__attribute__((address_space(3))) f32x4*)(lds + ((kq * 20 + mh * 10 + m * 5 + i) * 64 + lane) * 16) = acc[m][i];
    LGKM_BAR();
    for (int t = w; t < 20; t += 8) {
        f32x4 s = (lds_rdf4(lds + (t * 64 + lane) * 16) + lds_rdf4(lds + ((20 + t) * 64 + lane) * 16)) +
                  (lds_rdf4(lds + ((40 + t) * 64 + lane) * 16) + lds_rdf4(lds + ((60 + t) * 64 + lane) * 16));
        const int mt = t / 5, i = t - mt * 5;
        const int tk = mb * 64 + mt * 16 + l15, col = nb * 80 + i * 16 + g * 4;
        u32x2 xv = *(const u32x2*)(x + (size_t)tk * DM + col);
        u32x2 bv = *(const u32x2*)(b2 + col);
        u32x2 r;
        r[0] = pk2(s[0] + bf_lo(xv[0]) + bf_lo(bv[0]), s[1] + bf_hi(xv[0]) + bf_hi(bv[0]));
        r[1] = pk2(s[2] + bf_lo(xv[1]) + bf_lo(bv[1]), s[3] + bf_hi(xv[1]) + bf_hi(bv[1]));
        *(u32x2*)(out + (size_t)tk * DM + col) = r;
    }
}

extern "C" void kernel_launch(void* const* d_in, const int* in_sizes, int n_in, void* d_out, int out_size, void* d_ws, size_t ws_size,
                              hipStream_t stream) {
    const u16* x = (const u16*)d_in[0];
    const u16* wqkv = (const u16*)d_in[1];
    const u16* wo = (const u16*)d_in[2];
    const u16* w1 = (const u16*)d_in[4];
    const u16* b1 = (const u16*)d_in[5];
    const u16* w2 = (const u16*)d_in[6];
    const u16* b2 = (const u16*)d_in[7];
    char* ws = (char*)d_ws;
    k1_qkv_mlp1<<<256, 512, 0, stream>>>(x, wqkv, w1, b1, ws);
    k2_attn<<<256, 512, 0, stream>>>(ws);
    k3_out<<<256, 512, 0, stream>>>(x, wo, w2, b2, ws, (u16*)d_out);
}
```

```cpp
#include <hip/hip_runtime.h>
#include <stdint.h>

typedef __attribute__((ext_vector_type(8))) __bf16 bf16x8;
typedef __attribute__((ext_vector_type(2))) __bf16 bf16x2;
typedef __attribute__((ext_vector_type(4))) float f32x4;
typedef __attribute__((ext_vector_type(2))) float f32x2;
typedef __attribute__((ext_vector_type(16))) float f32x16;
typedef __attribute__((ext_vector_type(4))) unsigned u32x4;
typedef __attribute__((ext_vector_type(2))) unsigned u32x2;
typedef short v4i16_t __attribute__((ext_vector_type(4)));
typedef __attribute__((address_space(3))) char* lds_ptr;
typedef unsigned short u16;
template <int V> struct IC { static constexpr int v = V; };

#define SBAR() __builtin_amdgcn_sched_barrier(0)
#define PIN(x) asm volatile("" : "+v"(x))
#define WAIT_BAR(N) asm volatile("s_waitcnt vmcnt(" #N ") lgkmcnt(0)\n\ts_barrier" ::: "memory")
#define WAIT_VM(N) asm volatile("s_waitcnt vmcnt(" #N ")" ::: "memory")
#define LGKM_BAR() asm volatile("s_waitcnt lgkmcnt(0)\n\ts_barrier" ::: "memory")

#define PROF_LDS 0
#define STAMP(lds, base, i) do {} while (0)
#define STAMP_DUMP(ws, lds, base, kid, n) do {} while (0)
constexpr int SEQ = 2048, DM = 640, FF = 1536;
constexpr size_t WS_QK = 0;
constexpr size_t WS_V = WS_QK + (size_t)160 * SEQ * 16;
constexpr size_t WS_O = WS_V + (size_t)256 * DM * 16;
constexpr size_t WS_H = WS_O + (size_t)80 * SEQ * 16;
constexpr size_t WS_WT = WS_H + (size_t)192 * SEQ * 16;
constexpr size_t WS_P = WS_WT + (size_t)272 * DM * 16;
constexpr size_t WS_CNT = WS_P;
constexpr size_t WS_END = WS_P + (size_t)160 * SEQ * 16;

__device__ __forceinline__ void glds16(unsigned voff, const void* sbase, unsigned lds_base) {
    unsigned sv;
    asm volatile("s_mov_b32 %0, m0\n\ts_mov_b32 m0, %3\n\ts_nop 0\n\tglobal_load_lds_dwordx4 %1, %2\n\ts_mov_b32 m0, %0"
                 : "=&s"(sv) : "v"(voff), "s"(sbase), "s"(lds_base) : "memory");
}
__device__ __forceinline__ u32x2 tr_read(lds_ptr p) {
    return __builtin_bit_cast(u32x2, __builtin_amdgcn_ds_read_tr16_b64_v4i16((__attribute__((address_space(3))) v4i16_t*)p));
}
__device__ __forceinline__ u32x4 lds_rd128(lds_ptr p) { return *(__attribute__((address_space(3))) u32x4*)p; }
__device__ __forceinline__ f32x4 lds_rdf4(lds_ptr p) { return *(__attribute__((address_space(3))) f32x4*)p; }
__device__ __forceinline__ f32x2 lds_rdf2(lds_ptr p) { return *(__attribute__((address_space(3))) f32x2*)p; }
__device__ __forceinline__ float lds_rdf(lds_ptr p) { return *(__attribute__((address_space(3))) float*)p; }
__device__ __forceinline__ void lds_wrf4(lds_ptr p, f32x4 v) { *(__attribute__((address_space(3))) f32x4*)p = v; }
__device__ __forceinline__ void lds_wrf2(lds_ptr p, f32x2 v) { *(__attribute__((address_space(3))) f32x2*)p = v; }
__device__ __forceinline__ void lds_wrf(lds_ptr p, float v) { *(__attribute__((address_space(3))) float*)p = v; }
__device__ __forceinline__ f32x4 mfma16(u32x4 a, u32x4 b, f32x4 c) {
    return __builtin_amdgcn_mfma_f32_16x16x32_bf16(__builtin_bit_cast(bf16x8, a), __builtin_bit_cast(bf16x8, b), c, 0, 0, 0);
}
__device__ __forceinline__ f32x16 mfma32(u32x4 a, u32x4 b, f32x16 c) {
    return __builtin_amdgcn_mfma_f32_32x32x16_bf16(__builtin_bit_cast(bf16x8, a), __builtin_bit_cast(bf16x8, b), c, 0, 0, 0);
}
__device__ __forceinline__ unsigned pk2(float lo, float hi) {
    bf16x2 v = {(__bf16)lo, (__bf16)hi};
    return __builtin_bit_cast(unsigned, v);
}
__device__ __forceinline__ float bf_lo(unsigned u) { return __uint_as_float(u << 16); }
__device__ __forceinline__ float bf_hi(unsigned u) { return __uint_as_float(u & 0xffff0000u); }
constexpr unsigned ONES2 = 0x3f803f80u;

constexpr int K1_STAGE = 45056, K1_QOFF = 16384, K1_MOFF = 32768;
constexpr int K1_ROWST = 3 * K1_STAGE;
constexpr int K1_COLS = K1_ROWST + 2048;
constexpr int K1_TR = K1_COLS + 2048;
constexpr int K1_LDS = K1_TR + 16 * 1024;

__device__ __forceinline__ f32x2 gelu_tanh2(f32x2 u) {
    const float c1 = -2.0f * 0.7978845608028654f * 1.4426950408889634f;
    const float c2 = c1 * 0.044715f;
    f32x2 t = u * __builtin_elementwise_fma(u * u, f32x2{c2, c2}, f32x2{c1, c1});
    f32x2 d = f32x2{__builtin_amdgcn_exp2f(t[0]), __builtin_amdgcn_exp2f(t[1])} + f32x2{1.0f, 1.0f};
    return u * f32x2{__builtin_amdgcn_rcpf(d[0]), __builtin_amdgcn_rcpf(d[1])};
}

struct K1Dma { unsigned vx, vq, vm0, vm1, tv; const char *xg, *qg, *mg, *tsrc0, *tsrc1; char *tdst0, *tdst1; unsigned dst; bool t1; };

template <int TYPE, int WM>
__device__ __forceinline__ void k1_loop(lds_ptr lds, const K1Dma& dm, const unsigned (&xa)[2][2], const unsigned (&wa)[2][4],
                                        f32x4 (&acc)[4][4], f32x4 (&cs)[4], f32x4 (&st)[4]) {
    constexpr int NT = TYPE == 2 ? 3 : 4;
    constexpr int HI = TYPE == 2 ? 768 : 512;
    constexpr int NR = 4 + 2 * NT;
    const u32x4 ones = {ONES2, ONES2, ONES2, ONES2};
    u32x4 xf[2][4];
    u32x2 wl[2][4], wh[2][4];
    auto piece = [&](int j, int s, unsigned so) {
        const unsigned d = dm.dst + so + j * 8192;
        if (j == 0) glds16(dm.vx, dm.xg + s * 128, d);
        if (j == 1) glds16(dm.vx, dm.xg + s * 128 + 64 * DM * 2, d);
        if (j == 2) glds16(dm.vq, dm.qg + (size_t)s * (64 * 3 * DM * 2), d);
        if (j == 3) glds16(dm.vq, dm.qg + (size_t)s * (64 * 3 * DM * 2) + 32 * 3 * DM * 2, d);
        if (j == 4) glds16(dm.vm0, dm.mg + (size_t)s * (64 * FF * 2), d);
        if (j == 5 && WM == 0) glds16(dm.vm1, dm.mg + (size_t)s * (64 * FF * 2), d);
    };
    auto rd = [&](auto F_, int n, unsigned so) {
        constexpr int F = decltype(F_)::v;
        const int k = n == 0 ? 0 : n < 3 ? n + 3 : n < 6 ? n - 2 : n;
        if (k < 4) xf[F][k] = lds_rd128(lds + so + xa[F][k >> 1] + (k & 1) * 2048);
        else if (((k - 4) & 1) == 0) wl[F][(k - 4) >> 1] = tr_read(lds + so + wa[F][(k - 4) >> 1]);
        else wh[F][(k - 4) >> 1] = tr_read(lds + so + wa[F][(k - 4) >> 1] + HI);
    };
    auto mm = [&](auto F_, int n) {
        constexpr int F = decltype(F_)::v;
        if (n < 4 * NT) {
            const int i = n >> 2, m = n & 3;
            const u32x4 wf = {wl[F][i][0], wl[F][i][1], wh[F][i][0], wh[F][i][1]};
            acc[m][i] = TYPE == 1 ? mfma16(xf[F][m], wf, acc[m][i]) : mfma16(wf, xf[F][m], acc[m][i]);
        } else if (TYPE == 2 && n < 16) {
            const int m = n & 1;
            if (n < 14) st[m] = mfma16(ones, xf[F][m], st[m]);
            else st[2 + m] = mfma16(xf[F][m], xf[F][m], st[2 + m]);
        } else {
            const int i = n - 16;
            const u32x4 wf = {wl[F][i][0], wl[F][i][1], wh[F][i][0], wh[F][i][1]};
            cs[i] = TYPE == 1 ? mfma16(ones, wf, cs[i]) : mfma16(wf, ones, cs[i]);
        }
    };
    auto phase = [&](auto F_, auto RD_, auto DMA_, unsigned rso, int ds, unsigned dso) {
        constexpr int F = decltype(F_)::v;
        constexpr int NM = F == 0 ? 16 + NT : 16;
#pragma unroll
        for (int n = 0; n < NM; ++n) {
            mm(F_, n);
            if (decltype(RD_)::v && n < NR) rd(IC<1 - F>{}, n, rso);
            SBAR();
            if (decltype(DMA_)::v && n % 3 == 0 && n / 3 < 6) { piece(n / 3, ds, dso); SBAR(); }
        }
    };
#pragma unroll
    for (int s = 0; s < 2; ++s)
#pragma unroll
        for (int j = 0; j < 6; ++j) piece(j, s, s * K1_STAGE);
    STAMP(lds, K1_LDS, 15);
    if (WM == 0) { WAIT_BAR(6); } else { WAIT_BAR(5); }
    STAMP(lds, K1_LDS, 1);
#pragma unroll
    for (int n = 0; n < NR; ++n) rd(IC<0>{}, n, 0);
    phase(IC<0>{}, IC<1>{}, IC<1>{}, 0, 2, 2 * K1_STAGE);
    if (WM == 0) { WAIT_BAR(6); } else { WAIT_BAR(5); }
    unsigned so = 0;
    for (int s = 0; s < 7; ++s) {
        STAMP(lds, K1_LDS, 2 + s);
        const unsigned sn = so == 2 * K1_STAGE ? 0 : so + K1_STAGE;
        phase(IC<1>{}, IC<1>{}, IC<WM == 0>{}, sn, s + 3, so);
        phase(IC<0>{}, IC<1>{}, IC<WM == 1>{}, sn, s + 3, so);
        if (WM == 0) { WAIT_BAR(6); } else { WAIT_BAR(5); }
        so = sn;
    }
    for (int s = 7; s < 9; ++s) {
        STAMP(lds, K1_LDS, 2 + s);
        const unsigned sn = so == 2 * K1_STAGE ? 0 : so + K1_STAGE;
        phase(IC<1>{}, IC<1>{}, IC<0>{}, sn, 0, 0);
        phase(IC<0>{}, IC<1>{}, IC<0>{}, sn, 0, 0);
        if (s == 7) {
            WAIT_BAR(0);
            glds16(dm.tv, dm.tsrc0, dm.dst + K1_TR);
            glds16(dm.tv, dm.tsrc1, dm.dst + K1_TR + 8192);
        }
        so = sn;
    }
    STAMP(lds, K1_LDS, 11);
    phase(IC<1>{}, IC<0>{}, IC<0>{}, 0, 0, 0);
}

template <int TYPE>
__device__ __forceinline__ void k1_body(lds_ptr lds, const K1Dma& dm, int wm, int wn, int T0, const u16* x, const u16* b1, char* ws,
                                        int mb, int lane, bool live) {
    constexpr int NT = TYPE == 2 ? 3 : 4;
    const int g = lane >> 4, l15 = lane & 15;
    const int pm = TYPE == 2 ? 2 * (wn & 1) : 0;
    u32x2 bb[3];
    if constexpr (TYPE == 2) {
#pragma unroll
        for (int i = 0; i < 3; ++i) {
            const u16* bp = b1 + (T0 + i) * 16 + g * 4;
            asm volatile("global_load_dwordx2 %0, %1, off" : "=v"(bb[i]) : "v"(bp) : "memory");
        }
    }
    unsigned xa[2][2], wa[2][4];
    {
        const int t = wm * 64 + l15, f = (t >> 1) & 7;
        const int q = (lane >> 2) & 3, p = lane & 3;
#pragma unroll
        for (int F = 0; F < 2; ++F) {
            const int ks = wm ^ F;
            const unsigned xb = t * 128 + (((4 * ks + g) ^ f) * 16);
            xa[F][0] = xb + pm * 2048;
            xa[F][1] = xb + (2 - pm) * 2048;
#pragma unroll
            for (int a = 0; a < 4; ++a) {
                if (TYPE == 2) wa[F][a] = K1_MOFF + (ks * 4 + g) * 1536 + q * 192 + (((3 * (wn & 1) + a) ^ (g & 1)) * 32) + p * 8;
                else wa[F][a] = K1_QOFF + (ks * 4 + g) * 2048 + wn * 1024 + q * 128 + ((a ^ (((q >> 1) & 1) | ((g & 1) << 1))) * 32) + p * 8;
            }
        }
    }
    f32x4 acc[4][4], cs[4], st[4];
#pragma unroll
    for (int i = 0; i < 4; ++i) {
        cs[i] = f32x4{0.f, 0.f, 0.f, 0.f};
        st[i] = f32x4{0.f, 0.f, 0.f, 0.f};
#pragma unroll
        for (int m = 0; m < 4; ++m) acc[m][i] = f32x4{0.f, 0.f, 0.f, 0.f};
    }
    if (wm == 0) k1_loop<TYPE, 0>(lds, dm, xa, wa, acc, cs, st);
    else k1_loop<TYPE, 1>(lds, dm, xa, wa, acc, cs, st);
    STAMP(lds, K1_LDS, 12);
    {
        WAIT_VM(0);
        lds_ptr tp = (lds_ptr)(uintptr_t)dm.dst + K1_TR + ((lane >> 2) & 3) * 128 + g * 32 + (lane & 3) * 8;
        u32x2 lo = tr_read(tp), hi = tr_read(tp + 512);
        u32x2 lo1 = tr_read(tp + 8192), hi1 = tr_read(tp + 8192 + 512);
        *(u32x4*)(dm.tdst0 + lane * 16) = u32x4{lo[0], lo[1], hi[0], hi[1]};
        if (dm.t1) *(u32x4*)(dm.tdst1 + lane * 16) = u32x4{lo1[0], lo1[1], hi1[0], hi1[1]};
    }
    if constexpr (TYPE == 2) {
        if ((l15 >> 2) == g) {
#pragma unroll
            for (int m = 0; m < 2; ++m) {
                const int r = l15 & 3;
                const float rq = r == 0 ? st[2 + m][0] : r == 1 ? st[2 + m][1] : r == 2 ? st[2 + m][2] : st[2 + m][3];
                const int tl = wm * 64 + (pm + m) * 16 + l15;
                const float mu = st[m][0] * (1.0f / DM);
                float var = __builtin_fmaf(-mu, mu, rq * (1.0f / DM));
                if (!(var >= mu * mu * (1.0f / 256.0f))) {
                    const u16* xr = x + (size_t)(mb * 128 + tl) * DM;
                    float a = 0.f;
                    for (int k = 0; k < DM; ++k) {
                        float d = __uint_as_float((unsigned)xr[k] << 16) - mu;
                        a = __builtin_fmaf(d, d, a);
                    }
                    var = a * (1.0f / DM);
                }
                const float r1 = __builtin_amdgcn_rsqf(var + 1e-5f);
                const float r2 = r1 * __builtin_amdgcn_rsqf(__builtin_fmaf(var * r1, r1, 1e-5f));
                lds_wrf2(lds + K1_ROWST + tl * 8, f32x2{-mu * r1, r1});
                lds_wrf2(lds + K1_ROWST + 1024 + tl * 8, f32x2{-mu * r2, r2});
            }
        }
    }
    {
        lds_ptr cp = lds + K1_COLS + (wm * 4 + wn) * 256;
#pragma unroll
        for (int i = 0; i < NT; ++i) {
            if (TYPE == 1) { if (g == 0) lds_wrf(cp + i * 64 + l15 * 4, cs[i][0]); }
            else if (l15 == 0) lds_wrf4(cp + i * 64 + g * 16, cs[i]);
        }
    }
    LGKM_BAR();
    STAMP(lds, K1_LDS, 13);
    const int tok0 = mb * 128 + wm * 64;
    lds_ptr cp = lds + K1_COLS + ((1 - wm) * 4 + wn) * 256;
    if constexpr (TYPE == 1) {
        float c[4];
#pragma unroll
        for (int i = 0; i < 4; ++i) c[i] = cs[i][0] + lds_rdf(cp + i * 64 + l15 * 4);
#pragma unroll
        for (int mp = 0; mp < 2; ++mp) {
            f32x4 sa[2][2];
#pragma unroll
            for (int m = 0; m < 2; ++m) {
                sa[m][0] = lds_rdf4(lds + K1_ROWST + (wm * 64 + (2 * mp + m) * 16 + g * 4) * 8);
                sa[m][1] = lds_rdf4(lds + K1_ROWST + (wm * 64 + (2 * mp + m) * 16 + g * 4) * 8 + 16);
            }
            const int to = (tok0 >> 3) + mp * 4 + ((g & 1) * 2 + (g >> 1));
            char* vp = ws + WS_V + ((size_t)to * DM + (T0 - 80) * 16 + l15) * 16;
#pragma unroll
            for (int i = 0; i < 4; ++i) {
                unsigned pkd[2][2];
#pragma unroll
                for (int m = 0; m < 2; ++m) {
                    float v[4];
#pragma unroll
                    for (int r = 0; r < 4; ++r)
                        v[r] = __builtin_fmaf(acc[2 * mp + m][i][r], sa[m][r >> 1][(r & 1) * 2 + 1], sa[m][r >> 1][(r & 1) * 2] * c[i]);
                    pkd[m][0] = pk2(v[0], v[1]);
                    pkd[m][1] = pk2(v[2], v[3]);
                }
                auto s0 = __builtin_amdgcn_permlane16_swap(pkd[0][0], pkd[1][0], false, false);
                auto s1 = __builtin_amdgcn_permlane16_swap(pkd[0][1], pkd[1][1], false, false);
                if (live) *(u32x4*)(vp + i * 256) = u32x4{s0[0], s1[0], s0[1], s1[1]};
            }
        }
    } else {
        f32x2 sr[4];
        const float sc = (TYPE == 0 && T0 < 40) ? 0.22360679774997896f * 1.4426950408889634f : 1.0f;
#pragma unroll
        for (int m = 0; m < 4; ++m) {
            sr[m] = lds_rdf2(lds + K1_ROWST + (TYPE == 2 ? 1024 : 0) + (wm * 64 + (m ^ pm) * 16 + l15) * 8);
            if (TYPE == 0) sr[m] *= sc;
        }
        if constexpr (TYPE == 2) { PIN(bb[0]); PIN(bb[1]); PIN(bb[2]); }
        char* op = ws + (TYPE == 2 ? WS_H : WS_QK) + ((size_t)(2 * T0 + (g >> 1)) * SEQ + tok0 + (g & 1) * 16 + l15) * 16;
#pragma unroll
        for (int i = 0; i < NT; ++i) {
            const f32x4 c = cs[i] + lds_rdf4(cp + i * 64 + g * 16);
            const f32x2 c01 = {c[0], c[1]}, c23 = {c[2], c[3]};
            f32x2 b01 = {0.f, 0.f}, b23 = {0.f, 0.f};
            if constexpr (TYPE == 2) {
                b01 = f32x2{bf_lo(bb[i][0]), bf_hi(bb[i][0])};
                b23 = f32x2{bf_lo(bb[i][1]), bf_hi(bb[i][1])};
            }
#pragma unroll
            for (int mp = 0; mp < 2; ++mp) {
                unsigned pkd[2][2];
#pragma unroll
                for (int m = 0; m < 2; ++m) {
                    const f32x4 a = acc[2 * mp + m][i];
                    const f32x2 nm = {sr[2 * mp + m][0], sr[2 * mp + m][0]}, rr = {sr[2 * mp + m][1], sr[2 * mp + m][1]};
                    f32x2 u0, u1;
                    if constexpr (TYPE == 2) {
                        u0 = gelu_tanh2(__builtin_elementwise_fma(f32x2{a[0], a[1]}, rr, __builtin_elementwise_fma(nm, c01, b01)));
                        u1 = gelu_tanh2(__builtin_elementwise_fma(f32x2{a[2], a[3]}, rr, __builtin_elementwise_fma(nm, c23, b23)));
                    } else {
                        u0 = __builtin_elementwise_fma(f32x2{a[0], a[1]}, rr, nm * c01);
                        u1 = __builtin_elementwise_fma(f32x2{a[2], a[3]}, rr, nm * c23);
                    }
                    pkd[m][0] = pk2(u0[0], u0[1]);
                    pkd[m][1] = pk2(u1[0], u1[1]);
                }
                auto s0 = __builtin_amdgcn_permlane16_swap(pkd[0][0], pkd[1][0], false, false);
                auto s1 = __builtin_amdgcn_permlane16_swap(pkd[0][1], pkd[1][1], false, false);
                *(u32x4*)(op + (size_t)i * 2 * SEQ * 16 + ((2 * mp) ^ pm) * 256) = u32x4{s0[0], s1[0], s0[1], s1[1]};
            }
        }
    }
}

__global__ __launch_bounds__(512, 2) void k1_qkv_mlp1(const u16* __restrict__ x, const u16* __restrict__ wqkv, const u16* __restrict__ w1,
                                                      const u16* __restrict__ b1, const u16* __restrict__ wo, const u16* __restrict__ w2,
                                                      char* __restrict__ ws) {
    __shared__ __attribute__((aligned(1024))) char smem[K1_LDS + PROF_LDS];
    lds_ptr lds = (lds_ptr)smem;
    const unsigned lds0 = (unsigned)(uintptr_t)lds;
    const int lane = threadIdx.x & 63;
    const int w = __builtin_amdgcn_readfirstlane(threadIdx.x >> 6);
    const int b = blockIdx.x, xcd = b & 7, idx = b >> 3;
    const int mb = (xcd & 1) * 8 + (idx & 7), cg = (xcd >> 1) * 4 + (idx >> 3);
    const int cq = cg < 15 ? cg : 14;
    STAMP(lds, K1_LDS, 0);
    K1Dma dm;
    {
        const int q8 = lane >> 3, c = lane & 7;
        const int r = 8 * w + q8;
        dm.vx = r * (DM * 2) + ((c ^ ((r >> 1) & 7)) * 16);
        const int ko = w >> 1;
        const int a = (c >> 1) ^ (((q8 >> 1) & 1) | ((ko & 1) << 1));
        dm.vq = (8 * ko + q8) * (3 * DM * 2) + (w & 1) * 128 + a * 32 + (c & 1) * 16;
        unsigned vm[2];
#pragma unroll
        for (int i = 0; i < 2; ++i) {
            const int u = ((w + 8 * i) * 64 + lane) % 768;
            const int k2 = u / 96, rem = u - k2 * 96, row = rem / 12, c2 = rem - row * 12;
            vm[i] = (8 * k2 + row) * (FF * 2) + (((c2 >> 1) ^ (k2 & 1)) * 32) + (c2 & 1) * 16;
        }
        dm.vm0 = vm[0];
        dm.vm1 = vm[1];
        dm.xg = (const char*)x + (size_t)mb * 128 * DM * 2;
        dm.qg = (const char*)wqkv + cq * 256;
        dm.mg = (const char*)w1 + cg * 192;
        dm.dst = lds0 + w * 1024;
    }
    const int pid0 = b + 256 * w, pid1 = pid0 + 2048;
    const bool t1 = pid1 < 2720;
    const int pl0 = pid0 / 10, cb0 = pid0 - pl0 * 10, pl1 = t1 ? pid1 / 10 : 0, cb1 = t1 ? pid1 - pl1 * 10 : 0;
    dm.tv = (lane >> 3) * (DM * 2) + (lane & 7) * 16;
    dm.tsrc0 = (pl0 < 80 ? (const char*)wo + (size_t)pl0 * 8 * DM * 2 : (const char*)w2 + (size_t)(pl0 - 80) * 8 * DM * 2) + cb0 * 128;
    dm.tsrc1 = (const char*)w2 + (size_t)(t1 ? pl1 - 80 : 0) * 8 * DM * 2 + cb1 * 128;
    dm.tdst0 = ws + WS_WT + ((size_t)pl0 * DM + cb0 * 64) * 16;
    dm.tdst1 = ws + WS_WT + ((size_t)pl1 * DM + cb1 * 64) * 16;
    dm.t1 = t1;

    if (b == 0 && threadIdx.x < 8)
        __hip_atomic_store((__attribute__((address_space(1))) unsigned*)(ws + WS_CNT + threadIdx.x * 256), 0u, __ATOMIC_RELAXED, __HIP_MEMORY_SCOPE_AGENT);
    const int wm = w >> 2, wn = (w & 3) ^ (wm << 1);
    if (wn >= 2) k1_body<2>(lds, dm, wm, wn, 3 * (2 * cg + wn - 2), x, b1, ws, mb, lane, true);
    else if (cg >= 10) k1_body<1>(lds, dm, wm, wn, 4 * (2 * cq + wn), x, b1, ws, mb, lane, cg < 15);
    else k1_body<0>(lds, dm, wm, wn, 4 * (2 * cq + wn), x, b1, ws, mb, lane, true);
}

constexpr int K2_SLOT = 23552, K2_VOFF = 12288;
constexpr int K2_RING = 88 * 1024;
constexpr int K2_FLAG = K2_RING + 3 * K2_SLOT;
constexpr int K2_LDS = 160 * 1024;
typedef __attribute__((address_space(1))) unsigned gu32;

#define VSTAMP(i) do {} while (0)

__device__ __forceinline__ void glds16m(unsigned voff, const void* sbase, unsigned lds_base, unsigned long long mask) {
    unsigned sv;
    asm volatile("s_mov_b32 %0, m0\n\ts_mov_b32 m0, %3\n\ts_mov_b64 exec, %4\n\tglobal_load_lds_dwordx4 %1, %2\n\ts_mov_b64 exec, -1\n\ts_mov_b32 m0, %0"
                 : "=&s"(sv) : "v"(voff), "s"(sbase), "s"(lds_base), "s"(mask) : "memory");
}

template <int I, int N, class F> __device__ __forceinline__ void sfor(F&& f) {
    if constexpr (I < N) { f(IC<I>{}); sfor<I + 1, N>(f); }
}

__global__ __launch_bounds__(512, 2) void k2_attn(const u16* __restrict__ x, const u16* __restrict__ b2, char* __restrict__ ws,
                                                  u16* __restrict__ out) {
    __shared__ __attribute__((aligned(1024))) char smem[K2_LDS];
    lds_ptr lds = (lds_ptr)smem;
    const unsigned lds0 = (unsigned)(uintptr_t)lds;
    const int tid = threadIdx.x, lane = tid & 63, r32 = lane & 31, hi = lane >> 5;
    const int w = __builtin_amdgcn_readfirstlane(tid >> 6);
    const int b = blockIdx.x, xcd = b & 7, idx = b >> 3;
    const int h = xcd * 4 + (idx >> 3), qb = idx & 7;
    const int odd = h & 1, plane0 = 5 * (h >> 1) + 2 * odd;
    const int mp = odd, xp = odd ? 0 : 2;
    const int tok = qb * 256 + w * 32 + r32;
    VSTAMP(0);
    u32x4 q0, q1;
    {
        const char* qp0 = ws + WS_QK + ((size_t)(plane0 + mp + hi) * SEQ + tok) * 16;
        const char* qp1 = ws + WS_QK + ((size_t)(plane0 + xp) * SEQ + tok) * 16;
        asm volatile("global_load_dwordx4 %0, %1, off" : "=v"(q0) : "v"(qp0) : "memory");
        asm volatile("global_load_dwordx4 %0, %1, off" : "=v"(q1) : "v"(qp1) : "memory");
    }
    unsigned doff[3], dstr[3];
    unsigned long long dmask[3];
#pragma unroll
    for (int i = 0; i < 3; ++i) {
        const int n = w + 8 * i;
        const int gl = lane < 63 ? lane / 21 : 0, d = lane < 63 ? lane - gl * 21 : 0;
        doff[i] = n < 12 ? (unsigned)WS_QK + ((80 + plane0 + (n >> 2)) * SEQ + (n & 3) * 64 + lane) * 16
                         : (unsigned)WS_V + ((3 * (n - 12) + gl) * DM + 20 * h + d) * 16;
        dstr[i] = n < 12 ? 256 * 16 : 32 * DM * 16;
        dmask[i] = n < 12 ? ~0ull : n < 22 ? ~((1ull << 20) | (1ull << 41) | (3ull << 62)) : ((1ull << 41) - 1) & ~(1ull << 20);
    }
    const int kap = (r32 & 19) | ((r32 & 4) << 1) | ((r32 & 8) >> 1);
    const int mb = qb * 4 + (xcd & 3), nb = (xcd >> 2) * 4 + (idx >> 3), th = w & 1;
    const __amdgpu_buffer_rsrc_t rs = __builtin_amdgcn_make_buffer_rsrc(ws, 0, (unsigned)WS_END, 0x00020000);
    const unsigned hoff = ((lane >> 4) * SEQ + (lane & 15)) * 16, woff = ((lane >> 4) * DM + (lane & 15)) * 16;
    u32x4 fr[54];
    unsigned hbase, wbase;
    auto ldfrag = [&](auto F_) {
        constexpr int F = decltype(F_)::v, ks = F / 9, i = F % 9;
        if constexpr (i < 5) fr[F] = __builtin_amdgcn_raw_buffer_load_b128(rs, woff, wbase + ks * 4 * DM * 16 + i * 256, 0);
        else fr[F] = __builtin_amdgcn_raw_buffer_load_b128(rs, hoff, (hbase ^ (((i - 5) >> 1) * 512)) + ks * 4 * SEQ * 16 + ((i - 5) & 1) * 256, 0);
    };
    unsigned km = K2_RING + K2_SLOT + (mp + hi) * 4096 + kap * 16, kx = K2_RING + K2_SLOT + xp * 4096 + kap * 16, vb[3];
#pragma unroll
    for (int r = 0; r < 3; ++r) vb[r] = K2_RING + K2_SLOT + K2_VOFF + (r32 > 20 ? 1008 - 336 * r : r32 * 16 + hi * (r == 2 ? 352 : 336));
    f32x4 acc[4][5];
    auto kstep = [&](auto KS_, auto&& act) {
        constexpr int ks = decltype(KS_)::v;
        sfor<0, 20>([&](auto J_) {
            constexpr int j = decltype(J_)::v, m = j / 5, i = j % 5;
            acc[m][i] = mfma16(fr[9 * ks + i], fr[9 * ks + 5 + m], ks ? acc[m][i] : f32x4{0.f, 0.f, 0.f, 0.f});
            act(J_);
            SBAR();
        });
    };
    float mref = 0.f;
    const f32x16 zero = {0.f, 0.f, 0.f, 0.f, 0.f, 0.f, 0.f, 0.f, 0.f, 0.f, 0.f, 0.f, 0.f, 0.f, 0.f, 0.f};
    for (int att = 0;; ++att) {
        unsigned lw = lds0 + w * 1024;
        hbase = (unsigned)WS_H + (24 * w * SEQ + mb * 64 + th * 32) * 16;
        wbase = (unsigned)WS_WT + ((80 + 24 * w) * DM + nb * 80) * 16;
        asm volatile("" : "+s"(lw), "+s"(hbase), "+s"(wbase));
        auto piece = [&](int i, unsigned slot) {
            if (i < 2 || w < 7) glds16m(doff[i], ws, lw + K2_RING + slot + 8 * i * 1024, dmask[i]);
            doff[i] += dstr[i];
            PIN(doff[i]);
        };
        auto wopiece = [&](int r) {
            const unsigned p = 8 * r + w, ks = (p * 205u) >> 10, i = p - 5 * ks;
            glds16(woff, ws + WS_WT + (ks * 4 * DM + nb * 80 + i * 16) * 16, lw + r * 8192);
        };
#pragma unroll
        for (int c = 0; c < 2; ++c)
#pragma unroll
            for (int i = 0; i < 3; ++i) piece(i, (c + 1) * K2_SLOT);
        if (att == 0 && tid < 132) {
            const int sl = tid / 44, r = tid - sl * 44;
            unsigned one = (r & 3) == 3 ? 0u : ONES2;
            PIN(one);
            *(__attribute__((address_space(3))) u32x4*)(lds + K2_RING + sl * K2_SLOT + K2_VOFF + (r >> 2) * 1024 + ((r & 3) == 3 ? 1008 : 320 + (r & 3) * 336)) = u32x4{one, one, one, one};
        }
        if (tid == 0) *(__attribute__((address_space(3))) unsigned*)(lds + K2_FLAG) = 0u;
        asm volatile("s_waitcnt vmcnt(2) lgkmcnt(0)\n\ts_barrier" : "+v"(q0), "+v"(q1) :: "memory");
        VSTAMP(1);
        if (hi) q1 = u32x4{0u, 0u, 0u, 0u};
        else if (odd) { q1[0] = 0u; q1[1] = 0u; }
        else { q1[2] = 0u; q1[3] = 0u; }
        u32x4 kf0 = lds_rd128(lds + km), kf1 = lds_rd128(lds + kx), vf0, vf1, p0, p1;
        f32x16 s[2], negm, o;
        s[0] = mfma32(kf0, q0, zero);
        s[0] = mfma32(kf1, q1, s[0]);
        kf0 = lds_rd128(lds + km + 512);
        kf1 = lds_rd128(lds + kx + 512);
        vf0 = lds_rd128(lds + vb[0]);
        {
            unsigned z = 0u;
            PIN(z);
            vf1 = p1 = u32x4{z, z, z, z};
#pragma unroll
            for (int r = 0; r < 16; ++r) o[r] = __uint_as_float(z);
        }
        if (att == 0) {
            float m = s[0][0];
#pragma unroll
            for (int r = 1; r < 16; ++r) m = __builtin_fmaxf(m, s[0][r]);
            auto rr = __builtin_amdgcn_permlane32_swap(__float_as_uint(m), __float_as_uint(m), false, false);
            mref = __builtin_fmaxf(__uint_as_float(rr[0]), __uint_as_float(rr[1]));
        }
#pragma unroll
        for (int r = 0; r < 16; ++r) { negm[r] = -mref; s[0][r] -= mref; }
        PIN(negm);
        SBAR();
        auto tile = [&](auto TT_, auto KIND_, int c, unsigned step, unsigned nslot) {
            constexpr int TT = decltype(TT_)::v, KIND = decltype(KIND_)::v, CU = TT & 1, NX = CU ^ 1;
            auto extra = [&](auto G_) {
                constexpr int G = decltype(G_)::v;
                if constexpr (KIND == 0) {
                    if constexpr (G == 1 && TT == 1) wopiece(2 * c);
                    if constexpr (G == 1 && TT == 3) { if (c < 5) wopiece(2 * c + 1); }
                    if constexpr (TT >= 6 && (G & 1) && (TT - 6) * 2 + (G >> 1) < 3) piece((TT - 6) * 2 + (G >> 1), nslot);
                } else {
                    if constexpr (!(G & 1)) ldfrag(IC<(KIND - 1) * 16 + TT * 2 + (G >> 1)>{});
                    if constexpr (KIND == 1 && G == 1 && TT == 6) wopiece(11);
                    if constexpr (KIND == 1 && G == 1 && TT == 7) { if (w < 4) wopiece(12); }
                }
                SBAR();
            };
            auto soft = [&](auto G_) {
                constexpr int G = decltype(G_)::v;
                u32x4& p = G < 2 ? p0 : p1;
                float e0 = __builtin_amdgcn_exp2f(s[CU][4 * G]), e1 = __builtin_amdgcn_exp2f(s[CU][4 * G + 1]);
                PIN(e0); PIN(e1);
                SBAR();
                extra(G_);
                float e2 = __builtin_amdgcn_exp2f(s[CU][4 * G + 2]), e3 = __builtin_amdgcn_exp2f(s[CU][4 * G + 3]);
                PIN(e2); PIN(e3);
                SBAR();
                p[2 * (G & 1)] = pk2(e0, e1);
                p[2 * (G & 1) + 1] = pk2(e2, e3);
                PIN(p);
                SBAR();
            };
            if constexpr (TT == 6) {
                if constexpr (KIND == 0) asm volatile("s_waitcnt vmcnt(1)\n\ts_barrier" ::: "memory");
                else if constexpr (KIND == 1) asm volatile("s_waitcnt vmcnt(12)\n\ts_barrier" ::: "memory");
                km += step;
                kx += step;
                SBAR();
            }
            s[NX] = mfma32(kf0, q0, negm);
            kf0 = lds_rd128(lds + km + ((TT + 2) & 7) * 512);
            SBAR();
            soft(IC<0>{});
            s[NX] = mfma32(kf1, q1, s[NX]);
            kf1 = lds_rd128(lds + kx + ((TT + 2) & 7) * 512);
            SBAR();
            soft(IC<1>{});
            o = mfma32(vf1, p1, o);
            vf1 = lds_rd128(lds + vb[(4 * TT + 2) % 3] + ((4 * TT + 2) / 3) * 1024 + ((4 * TT + 2) % 3) * 336);
            SBAR();
            soft(IC<2>{});
            o = mfma32(vf0, p0, o);
            if constexpr (TT == 7) {
#pragma unroll
                for (int r = 0; r < 3; ++r) vb[r] += step;
            }
            constexpr int GN = (4 * TT + 4) & 31;
            vf0 = lds_rd128(lds + vb[GN % 3] + (GN / 3) * 1024 + (GN % 3) * 336);
            SBAR();
            soft(IC<3>{});
        };
        auto body = [&](auto KIND_, int c, unsigned step, unsigned nslot) {
            tile(IC<0>{}, KIND_, c, step, nslot); tile(IC<1>{}, KIND_, c, step, nslot); tile(IC<2>{}, KIND_, c, step, nslot);
            tile(IC<3>{}, KIND_, c, step, nslot); tile(IC<4>{}, KIND_, c, step, nslot); tile(IC<5>{}, KIND_, c, step, nslot);
            tile(IC<6>{}, KIND_, c, step, nslot); tile(IC<7>{}, KIND_, c, step, nslot);
        };
        unsigned nslot = 0;
#pragma unroll 1
        for (int c = 0; c < 6; ++c) {
            body(IC<0>{}, c, nslot == (unsigned)K2_SLOT ? (unsigned)(-2 * K2_SLOT) : (unsigned)K2_SLOT, nslot);
            nslot = nslot == 2u * K2_SLOT ? 0u : nslot + K2_SLOT;
            VSTAMP(2 + c);
        }
        body(IC<1>{}, 6, K2_SLOT, 0);
        VSTAMP(8);
        body(IC<2>{}, 7, -K2_SLOT, 0);
        VSTAMP(9);
        o = mfma32(vf1, p1, o);
        {
            auto rr = __builtin_amdgcn_permlane32_swap(__float_as_uint(o[8]), __float_as_uint(o[8]), false, false);
            const float l = __uint_as_float(rr[1]);
            bool ok = l <= 1.2676506e30f;
#pragma unroll
            for (int r = 0; r < 12; ++r) ok = ok && (__builtin_fabsf(o[r]) < __builtin_inff());
            if (!ok) *(__attribute__((address_space(3))) unsigned*)(lds + K2_FLAG) = 1u;
            const float inv = 1.0f / l;
            int ln = lane;
            PIN(ln);
            const unsigned ooff = (ln & 31) * 16 + (odd ? (ln >> 5 ? SEQ * 16 : 8) : (ln >> 5) * 8);
#pragma unroll
            for (int qd = 0; qd < 3; ++qd) {
                u32x2 v = {pk2(o[4 * qd] * inv, o[4 * qd + 1] * inv), pk2(o[4 * qd + 2] * inv, o[4 * qd + 3] * inv)};
                if (qd < 2 || ln < 32) __builtin_amdgcn_raw_buffer_store_b64(v, rs, ooff, (unsigned)WS_O + ((plane0 + qd) * SEQ + qb * 256 + w * 32) * 16, 16);
            }
        }
        SBAR();
        VSTAMP(18);
        kstep(IC<0>{}, [&](auto) {});
        VSTAMP(19);
        WAIT_BAR(0);
        if (att) break;
        const unsigned fl = __builtin_amdgcn_readfirstlane(*(__attribute__((address_space(3))) unsigned*)(lds + K2_FLAG));
        if (!fl) break;
        {
#pragma unroll
            for (int i = 0; i < 3; ++i) doff[i] -= 8 * dstr[i];
            float mx = -__builtin_inff();
            int ln = lane;
            PIN(ln);
#pragma unroll 1
            for (int t = 0; t < 64; ++t) {
                const u32x4 ka = __builtin_amdgcn_raw_buffer_load_b128(rs, ((ln >> 5) * SEQ + (ln & 31)) * 16, (unsigned)WS_QK + (80 + plane0 + mp) * SEQ * 16 + t * 512, 0);
                const u32x4 kb = __builtin_amdgcn_raw_buffer_load_b128(rs, (ln & 31) * 16, (unsigned)WS_QK + (80 + plane0 + xp) * SEQ * 16 + t * 512, 0);
                f32x16 z = mfma32(ka, q0, zero);
                z = mfma32(kb, q1, z);
#pragma unroll
                for (int r = 0; r < 16; ++r) mx = __builtin_fmaxf(mx, z[r]);
            }
            auto rr = __builtin_amdgcn_permlane32_swap(__float_as_uint(mx), __float_as_uint(mx), false, false);
            mref = __builtin_fmaxf(__uint_as_float(rr[0]), __uint_as_float(rr[1]));
        }
        WAIT_BAR(0);
    }
    gu32* cnt = (gu32*)(ws + WS_CNT + qb * 256);
    if (tid == 0) __hip_atomic_fetch_add(cnt, 1u, __ATOMIC_RELAXED, __HIP_MEMORY_SCOPE_AGENT);
    VSTAMP(10);
    int ln = lane;
    PIN(ln);
    kstep(IC<1>{}, [&](auto J_) { constexpr int j = decltype(J_)::v; if constexpr (j % 2 == 0) ldfrag(IC<32 + j / 2>{}); });
    VSTAMP(21);
    kstep(IC<2>{}, [&](auto J_) { constexpr int j = decltype(J_)::v; if constexpr (j % 3 == 0) ldfrag(IC<42 + j / 3>{}); });
    VSTAMP(22);
    kstep(IC<3>{}, [&](auto J_) { constexpr int j = decltype(J_)::v; if constexpr (j % 3 == 0 && j < 15) ldfrag(IC<49 + j / 3>{}); });
    VSTAMP(11);
    if (w == 0) {
        for (unsigned spins = 0; spins < (1u << 17); ++spins) {
            if ((unsigned)__builtin_amdgcn_readfirstlane(__hip_atomic_load(cnt, __ATOMIC_RELAXED, __HIP_MEMORY_SCOPE_AGENT)) >= 32u) break;
            __builtin_amdgcn_s_sleep(1);
        }
    }
    asm volatile("s_barrier" ::: "memory");
    VSTAMP(12);
    u32x4 of[5][2], wq[5][5];
    const unsigned obase = (unsigned)WS_O + (20 * (w >> 1) * SEQ + mb * 64 + th * 32) * 16;
    const unsigned wqb = (w >> 1) * 25600 + ln * 16;
    kstep(IC<4>{}, [&](auto J_) {
        constexpr int j = decltype(J_)::v;
        if constexpr (j % 2 == 0) of[(j / 2) % 5][j / 10] = __builtin_amdgcn_raw_buffer_load_b128(rs, hoff, obase + ((j / 2) % 5) * 4 * SEQ * 16 + (j / 10) * 256, 16);
        else if constexpr (j < 16) wq[j / 10][(j / 2) % 5] = lds_rd128(lds + wqb + (j / 2) * 1024);
    });
    VSTAMP(23);
    kstep(IC<5>{}, [&](auto J_) {
        constexpr int j = decltype(J_)::v;
        if constexpr (j < 9) wq[(8 + j) / 5][(8 + j) % 5] = lds_rd128(lds + wqb + (8 + j) * 1024);
    });
    sfor<17, 25>([&](auto J_) { constexpr int j = decltype(J_)::v; wq[j / 5][j % 5] = lds_rd128(lds + wqb + j * 1024); });
    asm volatile("s_waitcnt lgkmcnt(0)\n\ts_barrier" ::: "memory");
    VSTAMP(13);
    unsigned eoff[3];
    u32x2 xv[3], bv[3];
#pragma unroll
    for (int r = 0; r < 3; ++r) {
        const unsigned u = 160 * w + 64 * r + (r < 2 ? ln : ln & 31), sub = u >> 6, m = (sub * 205u) >> 10, i = sub - 5 * m;
        const unsigned col = nb * 80 + i * 16 + ((u >> 4) & 3) * 4;
        eoff[r] = ((mb * 64 + m * 16 + (u & 15)) * DM + col) * 2;
        xv[r] = *(const u32x2*)((const char*)x + eoff[r]);
        bv[r] = *(const u32x2*)((const char*)b2 + col * 2);
    }
    SBAR();
    const unsigned rw = w * 20480 + ln * 16, rwa = rw + (th ^ 1) * 10240, rwb = rw + th * 10240;
    sfor<0, 25>([&](auto J_) {
        constexpr int j = decltype(J_)::v;
        if constexpr (j % 2 == 0 && j < 20) lds_wrf4(lds + rwa + (j / 2) * 1024, acc[2 + j / 10][(j / 2) % 5]);
        acc[0][j % 5] = mfma16(wq[j / 5][j % 5], of[j / 5][0], acc[0][j % 5]);
        SBAR();
    });
    sfor<0, 25>([&](auto J_) {
        constexpr int j = decltype(J_)::v, i = j < 10 ? j % 2 : 2 + (j - 10) % 3, k = j < 10 ? j / 2 : (j - 10) / 3;
        acc[1][i] = mfma16(wq[k][i], of[k][1], acc[1][i]);
        if constexpr (j % 2 == 1 && j < 10) lds_wrf4(lds + rwb + (j / 2) * 1024, acc[0][j / 2]);
        if constexpr (j == 13 || j == 16) lds_wrf4(lds + rwb + (5 + (j - 13) / 3) * 1024, acc[1][(j - 13) / 3]);
        SBAR();
    });
    VSTAMP(14);
#pragma unroll
    for (int i = 2; i < 5; ++i) lds_wrf4(lds + rwb + (5 + i) * 1024, acc[1][i]);
    asm volatile("s_waitcnt lgkmcnt(0)\n\ts_barrier" ::: "memory");
    VSTAMP(15);
#pragma unroll
    for (int r = 0; r < 3; ++r) {
        lds_ptr p = lds + (160 * w + 64 * r + (r < 2 ? ln : ln & 31)) * 16;
        f32x4 t[8];
#pragma unroll
        for (int k = 0; k < 8; ++k) t[k] = lds_rdf4(p + k * 20480);
        const f32x4 s = ((t[0] + t[1]) + (t[2] + t[3])) + ((t[4] + t[5]) + (t[6] + t[7]));
        u32x2 v;
        v[0] = pk2(s[0] + bf_lo(xv[r][0]) + bf_lo(bv[r][0]), s[1] + bf_hi(xv[r][0]) + bf_hi(bv[r][0]));
        v[1] = pk2(s[2] + bf_lo(xv[r][1]) + bf_lo(bv[r][1]), s[3] + bf_hi(xv[r][1]) + bf_hi(bv[r][1]));
        if (r < 2 || ln < 32) *(u32x2*)((char*)out + eoff[r]) = v;
    }
}

extern "C" void kernel_launch(void* const* d_in, const int* in_sizes, int n_in, void* d_out, int out_size, void* d_ws, size_t ws_size,
                              hipStream_t stream) {
    const u16* x = (const u16*)d_in[0];
    const u16* wqkv = (const u16*)d_in[1];
    const u16* wo = (const u16*)d_in[2];
    const u16* w1 = (const u16*)d_in[4];
    const u16* b1 = (const u16*)d_in[5];
    const u16* w2 = (const u16*)d_in[6];
    const u16* b2 = (const u16*)d_in[7];
    char* ws = (char*)d_ws;
    k1_qkv_mlp1<<<256, 512, 0, stream>>>(x, wqkv, w1, b1, wo, w2, ws);
    k2_attn<<<256, 512, 0, stream>>>(x, b2, ws, (u16*)d_out);
}
```

```cpp
#include <hip/hip_runtime.h>
#include <stdint.h>

typedef __attribute__((ext_vector_type(8))) __bf16 bf16x8;
typedef __attribute__((ext_vector_type(2))) __bf16 bf16x2;
typedef __attribute__((ext_vector_type(4))) float f32x4;
typedef __attribute__((ext_vector_type(2))) float f32x2;
typedef __attribute__((ext_vector_type(16))) float f32x16;
typedef __attribute__((ext_vector_type(4))) unsigned u32x4;
typedef __attribute__((ext_vector_type(2))) unsigned u32x2;
typedef short v4i16_t __attribute__((ext_vector_type(4)));
typedef __attribute__((address_space(3))) char* lds_ptr;
typedef unsigned short u16;
template <int V> struct IC { static constexpr int v = V; };

#define SBAR() __builtin_amdgcn_sched_barrier(0)
#define PIN(x) asm volatile("" : "+v"(x))
#define WAIT_BAR(N) asm volatile("s_waitcnt vmcnt(" #N ") lgkmcnt(0)\n\ts_barrier" ::: "memory")
#define WAIT_VM(N) asm volatile("s_waitcnt vmcnt(" #N ")" ::: "memory")
#define LGKM_BAR() asm volatile("s_waitcnt lgkmcnt(0)\n\ts_barrier" ::: "memory")

#define PROF_LDS 0
#define STAMP(lds, base, i) do {} while (0)
#define STAMP_DUMP(ws, lds, base, kid, n) do {} while (0)
constexpr int SEQ = 2048, DM = 640, FF = 1536;
constexpr size_t WS_QK = 0;
constexpr size_t WS_V = WS_QK + (size_t)160 * SEQ * 16;
constexpr size_t WS_O = WS_V + (size_t)256 * DM * 16;
constexpr size_t WS_H = WS_O + (size_t)80 * SEQ * 16;
constexpr size_t WS_WT = WS_H + (size_t)192 * SEQ * 16;
constexpr size_t WS_P = WS_WT + (size_t)272 * DM * 16;
constexpr size_t WS_CNT = WS_P;
constexpr size_t WS_END = WS_P + (size_t)160 * SEQ * 16;

__device__ __forceinline__ void glds16(unsigned voff, const void* sbase, unsigned lds_base) {
    unsigned sv;
    asm volatile("s_mov_b32 %0, m0\n\ts_mov_b32 m0, %3\n\ts_nop 0\n\tglobal_load_lds_dwordx4 %1, %2\n\ts_mov_b32 m0, %0"
                 : "=&s"(sv) : "v"(voff), "s"(sbase), "s"(lds_base) : "memory");
}
__device__ __forceinline__ u32x2 tr_read(lds_ptr p) {
    return __builtin_bit_cast(u32x2, __builtin_amdgcn_ds_read_tr16_b64_v4i16((__attribute__((address_space(3))) v4i16_t*)p));
}
__device__ __forceinline__ u32x4 lds_rd128(lds_ptr p) { return *(__attribute__((address_space(3))) u32x4*)p; }
__device__ __forceinline__ f32x4 lds_rdf4(lds_ptr p) { return *(__attribute__((address_space(3))) f32x4*)p; }
__device__ __forceinline__ f32x2 lds_rdf2(lds_ptr p) { return *(__attribute__((address_space(3))) f32x2*)p; }
__device__ __forceinline__ float lds_rdf(lds_ptr p) { return *(__attribute__((address_space(3))) float*)p; }
__device__ __forceinline__ void lds_wrf4(lds_ptr p, f32x4 v) { *(__attribute__((address_space(3))) f32x4*)p = v; }
__device__ __forceinline__ void lds_wrf2(lds_ptr p, f32x2 v) { *(__attribute__((address_space(3))) f32x2*)p = v; }
__device__ __forceinline__ void lds_wrf(lds_ptr p, float v) { *(__attribute__((address_space(3))) float*)p = v; }
__device__ __forceinline__ f32x4 mfma16(u32x4 a, u32x4 b, f32x4 c) {
    return __builtin_amdgcn_mfma_f32_16x16x32_bf16(__builtin_bit_cast(bf16x8, a), __builtin_bit_cast(bf16x8, b), c, 0, 0, 0);
}
__device__ __forceinline__ f32x16 mfma32(u32x4 a, u32x4 b, f32x16 c) {
    return __builtin_amdgcn_mfma_f32_32x32x16_bf16(__builtin_bit_cast(bf16x8, a), __builtin_bit_cast(bf16x8, b), c, 0, 0, 0);
}
__device__ __forceinline__ unsigned pk2(float lo, float hi) {
    bf16x2 v = {(__bf16)lo, (__bf16)hi};
    return __builtin_bit_cast(unsigned, v);
}
__device__ __forceinline__ float bf_lo(unsigned u) { return __uint_as_float(u << 16); }
__device__ __forceinline__ float bf_hi(unsigned u) { return __uint_as_float(u & 0xffff0000u); }
constexpr unsigned ONES2 = 0x3f803f80u;

constexpr int K1_STAGE = 45056, K1_QOFF = 16384, K1_MOFF = 32768;
constexpr int K1_ROWST = 3 * K1_STAGE;
constexpr int K1_COLS = K1_ROWST + 2048;
constexpr int K1_TR = K1_COLS + 2048;
constexpr int K1_LDS = K1_TR + 16 * 1024;

__device__ __forceinline__ f32x2 gelu_tanh2(f32x2 u) {
    const float c1 = -2.0f * 0.7978845608028654f * 1.4426950408889634f;
    const float c2 = c1 * 0.044715f;
    f32x2 t = u * __builtin_elementwise_fma(u * u, f32x2{c2, c2}, f32x2{c1, c1});
    f32x2 d = f32x2{__builtin_amdgcn_exp2f(t[0]), __builtin_amdgcn_exp2f(t[1])} + f32x2{1.0f, 1.0f};
    return u * f32x2{__builtin_amdgcn_rcpf(d[0]), __builtin_amdgcn_rcpf(d[1])};
}

struct K1Dma { unsigned vx, vq, vm0, vm1, tv; const char *xg, *qg, *mg, *tsrc0, *tsrc1; char *tdst0, *tdst1; unsigned dst; bool t1; };

template <int TYPE, int WM>
__device__ __forceinline__ void k1_loop(lds_ptr lds, const K1Dma& dm, const unsigned (&xa)[2][2], const unsigned (&wa)[2][4],
                                        f32x4 (&acc)[4][4], f32x4 (&cs)[4], f32x4 (&st)[4], const u16* bp, u32x2 (&bb)[3]) {
    constexpr int NT = TYPE == 2 ? 3 : 4;
    constexpr int HI = TYPE == 2 ? 768 : 512;
    constexpr int NR = 4 + 2 * NT;
    const u32x4 ones = {ONES2, ONES2, ONES2, ONES2};
    u32x4 xf[2][4];
    u32x2 wl[2][4], wh[2][4];
    auto piece = [&](int j, int s, unsigned so) {
        const unsigned d = dm.dst + so + j * 8192;
        if (j == 0) glds16(dm.vx, dm.xg + s * 128, d);
        if (j == 1) glds16(dm.vx, dm.xg + s * 128 + 64 * DM * 2, d);
        if (j == 2) glds16(dm.vq, dm.qg + (size_t)s * (64 * 3 * DM * 2), d);
        if (j == 3) glds16(dm.vq, dm.qg + (size_t)s * (64 * 3 * DM * 2) + 32 * 3 * DM * 2, d);
        if (j == 4) glds16(dm.vm0, dm.mg + (size_t)s * (64 * FF * 2), d);
        if (j == 5 && WM == 0) glds16(dm.vm1, dm.mg + (size_t)s * (64 * FF * 2), d);
    };
    auto rd = [&](auto F_, int n, unsigned so) {
        constexpr int F = decltype(F_)::v;
        const int k = n == 0 ? 0 : n < 3 ? n + 3 : n < 6 ? n - 2 : n;
        if (k < 4) xf[F][k] = lds_rd128(lds + so + xa[F][k >> 1] + (k & 1) * 2048);
        else if (((k - 4) & 1) == 0) wl[F][(k - 4) >> 1] = tr_read(lds + so + wa[F][(k - 4) >> 1]);
        else wh[F][(k - 4) >> 1] = tr_read(lds + so + wa[F][(k - 4) >> 1] + HI);
    };
    auto mm = [&](auto F_, int n) {
        constexpr int F = decltype(F_)::v;
        if (n < 4 * NT) {
            const int i = n >> 2, m = n & 3;
            const u32x4 wf = {wl[F][i][0], wl[F][i][1], wh[F][i][0], wh[F][i][1]};
            acc[m][i] = TYPE == 1 ? mfma16(xf[F][m], wf, acc[m][i]) : mfma16(wf, xf[F][m], acc[m][i]);
        } else if (TYPE == 2 && n < 16) {
            const int m = n & 1;
            if (n < 14) st[m] = mfma16(ones, xf[F][m], st[m]);
            else st[2 + m] = mfma16(xf[F][m], xf[F][m], st[2 + m]);
        } else {
            const int i = n - 16;
            const u32x4 wf = {wl[F][i][0], wl[F][i][1], wh[F][i][0], wh[F][i][1]};
            cs[i] = TYPE == 1 ? mfma16(ones, wf, cs[i]) : mfma16(wf, ones, cs[i]);
        }
    };
    auto phase = [&](auto F_, auto RD_, auto DMA_, unsigned rso, int ds, unsigned dso) {
        constexpr int F = decltype(F_)::v;
        constexpr int NM = F == 0 ? 16 + NT : 16;
#pragma unroll
        for (int n = 0; n < NM; ++n) {
            mm(F_, n);
            if (decltype(RD_)::v && n < NR) rd(IC<1 - F>{}, n, rso);
            SBAR();
            if (decltype(DMA_)::v && n % 3 == 0 && n / 3 < 6) { piece(n / 3, ds, dso); SBAR(); }
        }
    };
    STAMP(lds, K1_LDS, 15);
    if (WM == 0) { WAIT_BAR(6); } else { WAIT_BAR(5); }
    STAMP(lds, K1_LDS, 1);
#pragma unroll
    for (int n = 0; n < NR; ++n) rd(IC<0>{}, n, 0);
    phase(IC<0>{}, IC<1>{}, IC<1>{}, 0, 2, 2 * K1_STAGE);
    if (WM == 0) { WAIT_BAR(6); } else { WAIT_BAR(5); }
    unsigned so = 0;
    for (int s = 0; s < 7; ++s) {
        STAMP(lds, K1_LDS, 2 + s);
        const unsigned sn = so == 2 * K1_STAGE ? 0 : so + K1_STAGE;
        phase(IC<1>{}, IC<1>{}, IC<WM == 0>{}, sn, s + 3, so);
        phase(IC<0>{}, IC<1>{}, IC<WM == 1>{}, sn, s + 3, so);
        if (WM == 0) { WAIT_BAR(6); } else { WAIT_BAR(5); }
        so = sn;
    }
    for (int s = 7; s < 9; ++s) {
        STAMP(lds, K1_LDS, 2 + s);
        const unsigned sn = so == 2 * K1_STAGE ? 0 : so + K1_STAGE;
        phase(IC<1>{}, IC<1>{}, IC<0>{}, sn, 0, 0);
        phase(IC<0>{}, IC<1>{}, IC<0>{}, sn, 0, 0);
        if (s == 7) {
            WAIT_BAR(0);
            glds16(dm.tv, dm.tsrc0, dm.dst + K1_TR);
            glds16(dm.tv, dm.tsrc1, dm.dst + K1_TR + 8192);
            if constexpr (TYPE == 2) {
                asm volatile("global_load_dwordx2 %0, %3, off\n\tglobal_load_dwordx2 %1, %3, off offset:32\n\tglobal_load_dwordx2 %2, %3, off offset:64"
                             : "=&v"(bb[0]), "=&v"(bb[1]), "=&v"(bb[2]) : "v"(bp) : "memory");
            }
        }
        so = sn;
    }
    STAMP(lds, K1_LDS, 11);
    phase(IC<1>{}, IC<0>{}, IC<0>{}, 0, 0, 0);
    if constexpr (TYPE == 2) asm volatile("s_waitcnt vmcnt(0)" : "+v"(bb[0]), "+v"(bb[1]), "+v"(bb[2]) :: "memory");
}

template <int TYPE>
__device__ __forceinline__ void k1_body(lds_ptr lds, const K1Dma& dm, int wm, int wn, int T0, const u16* x, const u16* b1, char* ws,
                                        int mb, int lane, bool live) {
    constexpr int NT = TYPE == 2 ? 3 : 4;
    const int g = lane >> 4, l15 = lane & 15;
    const int pm = TYPE == 2 ? 2 * (wn & 1) : 0;
    u32x2 bb[3];
    const u16* bp = b1 + T0 * 16 + g * 4;
    unsigned xa[2][2], wa[2][4];
    {
        const int t = wm * 64 + l15, f = (t >> 1) & 7;
        const int q = (lane >> 2) & 3, p = lane & 3;
#pragma unroll
        for (int F = 0; F < 2; ++F) {
            const int ks = wm ^ F;
            const unsigned xb = t * 128 + (((4 * ks + g) ^ f) * 16);
            xa[F][0] = xb + pm * 2048;
            xa[F][1] = xb + (2 - pm) * 2048;
#pragma unroll
            for (int a = 0; a < 4; ++a) {
                if (TYPE == 2) wa[F][a] = K1_MOFF + (ks * 4 + g) * 1536 + q * 192 + (((3 * (wn & 1) + a) ^ (g & 1)) * 32) + p * 8;
                else wa[F][a] = K1_QOFF + (ks * 4 + g) * 2048 + wn * 1024 + q * 128 + ((a ^ (((q >> 1) & 1) | ((g & 1) << 1))) * 32) + p * 8;
            }
        }
    }
    f32x4 acc[4][4], cs[4], st[4];
#pragma unroll
    for (int i = 0; i < 4; ++i) {
        cs[i] = f32x4{0.f, 0.f, 0.f, 0.f};
        st[i] = f32x4{0.f, 0.f, 0.f, 0.f};
#pragma unroll
        for (int m = 0; m < 4; ++m) acc[m][i] = f32x4{0.f, 0.f, 0.f, 0.f};
    }
    if (wm == 0) k1_loop<TYPE, 0>(lds, dm, xa, wa, acc, cs, st, bp, bb);
    else k1_loop<TYPE, 1>(lds, dm, xa, wa, acc, cs, st, bp, bb);
    STAMP(lds, K1_LDS, 12);
    {
        WAIT_VM(0);
        lds_ptr tp = (lds_ptr)(uintptr_t)dm.dst + K1_TR + ((lane >> 2) & 3) * 128 + g * 32 + (lane & 3) * 8;
        u32x2 lo = tr_read(tp), hi = tr_read(tp + 512);
        u32x2 lo1 = tr_read(tp + 8192), hi1 = tr_read(tp + 8192 + 512);
        *(u32x4*)(dm.tdst0 + lane * 16) = u32x4{lo[0], lo[1], hi[0], hi[1]};
        if (dm.t1) *(u32x4*)(dm.tdst1 + lane * 16) = u32x4{lo1[0], lo1[1], hi1[0], hi1[1]};
    }
    if constexpr (TYPE == 2) {
        if ((l15 >> 2) == g) {
#pragma unroll
            for (int m = 0; m < 2; ++m) {
                const int r = l15 & 3;
                const float rq = r == 0 ? st[2 + m][0] : r == 1 ? st[2 + m][1] : r == 2 ? st[2 + m][2] : st[2 + m][3];
                const int tl = wm * 64 + (pm + m) * 16 + l15;
                const float mu = st[m][0] * (1.0f / DM);
                float var = __builtin_fmaf(-mu, mu, rq * (1.0f / DM));
                if (!(var >= mu * mu * (1.0f / 256.0f))) {
                    const u16* xr = x + (size_t)(mb * 128 + tl) * DM;
                    float a = 0.f;
                    for (int k = 0; k < DM; ++k) {
                        float d = __uint_as_float((unsigned)xr[k] << 16) - mu;
                        a = __builtin_fmaf(d, d, a);
                    }
                    var = a * (1.0f / DM);
                }
                const float r1 = __builtin_amdgcn_rsqf(var + 1e-5f);
                const float r2 = r1 * __builtin_amdgcn_rsqf(__builtin_fmaf(var * r1, r1, 1e-5f));
                lds_wrf2(lds + K1_ROWST + tl * 8, f32x2{-mu * r1, r1});
                lds_wrf2(lds + K1_ROWST + 1024 + tl * 8, f32x2{-mu * r2, r2});
            }
        }
    }
    {
        lds_ptr cp = lds + K1_COLS + (wm * 4 + wn) * 256;
#pragma unroll
        for (int i = 0; i < NT; ++i) {
            if (TYPE == 1) { if (g == 0) lds_wrf(cp + i * 64 + l15 * 4, cs[i][0]); }
            else if (l15 == 0) lds_wrf4(cp + i * 64 + g * 16, cs[i]);
        }
    }
    LGKM_BAR();
    STAMP(lds, K1_LDS, 13);
    const int tok0 = mb * 128 + wm * 64;
    lds_ptr cp = lds + K1_COLS + ((1 - wm) * 4 + wn) * 256;
    if constexpr (TYPE == 1) {
        float c[4];
#pragma unroll
        for (int i = 0; i < 4; ++i) c[i] = cs[i][0] + lds_rdf(cp + i * 64 + l15 * 4);
#pragma unroll
        for (int mp = 0; mp < 2; ++mp) {
            f32x4 sa[2][2];
#pragma unroll
            for (int m = 0; m < 2; ++m) {
                sa[m][0] = lds_rdf4(lds + K1_ROWST + (wm * 64 + (2 * mp + m) * 16 + g * 4) * 8);
                sa[m][1] = lds_rdf4(lds + K1_ROWST + (wm * 64 + (2 * mp + m) * 16 + g * 4) * 8 + 16);
            }
            const int to = (tok0 >> 3) + mp * 4 + ((g & 1) * 2 + (g >> 1));
            char* vp = ws + WS_V + ((size_t)to * DM + (T0 - 80) * 16 + l15) * 16;
#pragma unroll
            for (int i = 0; i < 4; ++i) {
                unsigned pkd[2][2];
#pragma unroll
                for (int m = 0; m < 2; ++m) {
                    float v[4];
#pragma unroll
                    for (int r = 0; r < 4; ++r)
                        v[r] = __builtin_fmaf(acc[2 * mp + m][i][r], sa[m][r >> 1][(r & 1) * 2 + 1], sa[m][r >> 1][(r & 1) * 2] * c[i]);
                    pkd[m][0] = pk2(v[0], v[1]);
                    pkd[m][1] = pk2(v[2], v[3]);
                }
                auto s0 = __builtin_amdgcn_permlane16_swap(pkd[0][0], pkd[1][0], false, false);
                auto s1 = __builtin_amdgcn_permlane16_swap(pkd[0][1], pkd[1][1], false, false);
                if (live) *(u32x4*)(vp + i * 256) = u32x4{s0[0], s1[0], s0[1], s1[1]};
            }
        }
    } else {
        f32x2 sr[4];
        const float sc = (TYPE == 0 && T0 < 40) ? 0.22360679774997896f * 1.4426950408889634f : 1.0f;
#pragma unroll
        for (int m = 0; m < 4; ++m) {
            sr[m] = lds_rdf2(lds + K1_ROWST + (TYPE == 2 ? 1024 : 0) + (wm * 64 + (m ^ pm) * 16 + l15) * 8);
            if (TYPE == 0) sr[m] *= sc;
        }
        if constexpr (TYPE == 2) { PIN(bb[0]); PIN(bb[1]); PIN(bb[2]); }
        char* op = ws + (TYPE == 2 ? WS_H : WS_QK) + ((size_t)(2 * T0 + (g >> 1)) * SEQ + tok0 + (g & 1) * 16 + l15) * 16;
#pragma unroll
        for (int i = 0; i < NT; ++i) {
            const f32x4 c = cs[i] + lds_rdf4(cp + i * 64 + g * 16);
            const f32x2 c01 = {c[0], c[1]}, c23 = {c[2], c[3]};
            f32x2 b01 = {0.f, 0.f}, b23 = {0.f, 0.f};
            if constexpr (TYPE == 2) {
                b01 = f32x2{bf_lo(bb[i][0]), bf_hi(bb[i][0])};
                b23 = f32x2{bf_lo(bb[i][1]), bf_hi(bb[i][1])};
            }
#pragma unroll
            for (int mp = 0; mp < 2; ++mp) {
                unsigned pkd[2][2];
#pragma unroll
                for (int m = 0; m < 2; ++m) {
                    const f32x4 a = acc[2 * mp + m][i];
                    const f32x2 nm = {sr[2 * mp + m][0], sr[2 * mp + m][0]}, rr = {sr[2 * mp + m][1], sr[2 * mp + m][1]};
                    f32x2 u0, u1;
                    if constexpr (TYPE == 2) {
                        u0 = gelu_tanh2(__builtin_elementwise_fma(f32x2{a[0], a[1]}, rr, __builtin_elementwise_fma(nm, c01, b01)));
                        u1 = gelu_tanh2(__builtin_elementwise_fma(f32x2{a[2], a[3]}, rr, __builtin_elementwise_fma(nm, c23, b23)));
                    } else {
                        u0 = __builtin_elementwise_fma(f32x2{a[0], a[1]}, rr, nm * c01);
                        u1 = __builtin_elementwise_fma(f32x2{a[2], a[3]}, rr, nm * c23);
                    }
                    pkd[m][0] = pk2(u0[0], u0[1]);
                    pkd[m][1] = pk2(u1[0], u1[1]);
                }
                auto s0 = __builtin_amdgcn_permlane16_swap(pkd[0][0], pkd[1][0], false, false);
                auto s1 = __builtin_amdgcn_permlane16_swap(pkd[0][1], pkd[1][1], false, false);
                *(u32x4*)(op + (size_t)i * 2 * SEQ * 16 + ((2 * mp) ^ pm) * 256) = u32x4{s0[0], s1[0], s0[1], s1[1]};
            }
        }
    }
}

__global__ __launch_bounds__(512, 2) void k1_qkv_mlp1(const u16* __restrict__ x, const u16* __restrict__ wqkv, const u16* __restrict__ w1,
                                                      const u16* __restrict__ b1, const u16* __restrict__ wo, const u16* __restrict__ w2,
                                                      char* __restrict__ ws) {
    __shared__ __attribute__((aligned(1024))) char smem[K1_LDS + PROF_LDS];
    lds_ptr lds = (lds_ptr)smem;
    const unsigned lds0 = (unsigned)(uintptr_t)lds;
    const int lane = threadIdx.x & 63;
    const int w = __builtin_amdgcn_readfirstlane(threadIdx.x >> 6);
    const int b = blockIdx.x, xcd = b & 7, idx = b >> 3;
    const int mb = (xcd & 1) * 8 + (idx & 7), cg = (xcd >> 1) * 4 + (idx >> 3);
    const int cq = cg < 15 ? cg : 14;
    STAMP(lds, K1_LDS, 0);
    K1Dma dm;
    {
        dm.dst = lds0 + w * 1024;
        const int q8 = lane >> 3, c = lane & 7;
        const int r = 8 * w + q8;
        dm.vx = r * (DM * 2) + ((c ^ ((r >> 1) & 7)) * 16);
        dm.xg = (const char*)x + (size_t)mb * 128 * DM * 2;
        glds16(dm.vx, dm.xg, dm.dst);
        glds16(dm.vx, dm.xg + 64 * DM * 2, dm.dst + 8192);
        const int ko = w >> 1;
        const int a = (c >> 1) ^ (((q8 >> 1) & 1) | ((ko & 1) << 1));
        dm.vq = (8 * ko + q8) * (3 * DM * 2) + (w & 1) * 128 + a * 32 + (c & 1) * 16;
        dm.qg = (const char*)wqkv + cq * 256;
        glds16(dm.vq, dm.qg, dm.dst + 2 * 8192);
        glds16(dm.vq, dm.qg + 32 * 3 * DM * 2, dm.dst + 3 * 8192);
        unsigned vm[2];
#pragma unroll
        for (int i = 0; i < 2; ++i) {
            const int u = ((w + 8 * i) * 64 + lane) % 768;
            const int k2 = u / 96, rem = u - k2 * 96, row = rem / 12, c2 = rem - row * 12;
            vm[i] = (8 * k2 + row) * (FF * 2) + (((c2 >> 1) ^ (k2 & 1)) * 32) + (c2 & 1) * 16;
        }
        dm.vm0 = vm[0];
        dm.vm1 = vm[1];
        dm.mg = (const char*)w1 + cg * 192;
        glds16(dm.vm0, dm.mg, dm.dst + 4 * 8192);
        if (w < 4) glds16(dm.vm1, dm.mg, dm.dst + 5 * 8192);
        glds16(dm.vx, dm.xg + 128, dm.dst + K1_STAGE);
        glds16(dm.vx, dm.xg + 128 + 64 * DM * 2, dm.dst + K1_STAGE + 8192);
        glds16(dm.vq, dm.qg + 64 * 3 * DM * 2, dm.dst + K1_STAGE + 2 * 8192);
        glds16(dm.vq, dm.qg + 96 * 3 * DM * 2, dm.dst + K1_STAGE + 3 * 8192);
        glds16(dm.vm0, dm.mg + 64 * FF * 2, dm.dst + K1_STAGE + 4 * 8192);
        if (w < 4) glds16(dm.vm1, dm.mg + 64 * FF * 2, dm.dst + K1_STAGE + 5 * 8192);
    }
    const int pid0 = b + 256 * w, pid1 = pid0 + 2048;
    const bool t1 = pid1 < 2720;
    const int pl0 = pid0 / 10, cb0 = pid0 - pl0 * 10, pl1 = t1 ? pid1 / 10 : 0, cb1 = t1 ? pid1 - pl1 * 10 : 0;
    dm.tv = (lane >> 3) * (DM * 2) + (lane & 7) * 16;
    dm.tsrc0 = (pl0 < 80 ? (const char*)wo + (size_t)pl0 * 8 * DM * 2 : (const char*)w2 + (size_t)(pl0 - 80) * 8 * DM * 2) + cb0 * 128;
    dm.tsrc1 = (const char*)w2 + (size_t)(t1 ? pl1 - 80 : 0) * 8 * DM * 2 + cb1 * 128;
    dm.tdst0 = ws + WS_WT + ((size_t)pl0 * DM + cb0 * 64) * 16;
    dm.tdst1 = ws + WS_WT + ((size_t)pl1 * DM + cb1 * 64) * 16;
    dm.t1 = t1;

    const int wm = w >> 2, wn = (w & 3) ^ (wm << 1);
    if (wn >= 2) k1_body<2>(lds, dm, wm, wn, 3 * (2 * cg + wn - 2), x, b1, ws, mb, lane, true);
    else if (cg >= 10) k1_body<1>(lds, dm, wm, wn, 4 * (2 * cq + wn), x, b1, ws, mb, lane, cg < 15);
    else k1_body<0>(lds, dm, wm, wn, 4 * (2 * cq + wn), x, b1, ws, mb, lane, true);
    if (b == 0 && threadIdx.x < 8)
        __hip_atomic_store((__attribute__((address_space(1))) unsigned*)(ws + WS_CNT + threadIdx.x * 256), 0u, __ATOMIC_RELAXED, __HIP_MEMORY_SCOPE_AGENT);
}

constexpr int K2_SLOT = 23552, K2_VOFF = 12288;
constexpr int K2_RING = 88 * 1024;
constexpr int K2_FLAG = K2_RING + 3 * K2_SLOT;
constexpr int K2_LDS = 160 * 1024;
typedef __attribute__((address_space(1))) unsigned gu32;

#define VSTAMP(i) do {} while (0)

__device__ __forceinline__ void glds16m(unsigned voff, const void* sbase, unsigned lds_base, unsigned long long mask) {
    unsigned sv;
    asm volatile("s_mov_b32 %0, m0\n\ts_mov_b32 m0, %3\n\ts_mov_b64 exec, %4\n\tglobal_load_lds_dwordx4 %1, %2\n\ts_mov_b64 exec, -1\n\ts_mov_b32 m0, %0"
                 : "=&s"(sv) : "v"(voff), "s"(sbase), "s"(lds_base), "s"(mask) : "memory");
}

template <int I, int N, class F> __device__ __forceinline__ void sfor(F&& f) {
    if constexpr (I < N) { f(IC<I>{}); sfor<I + 1, N>(f); }
}

__global__ __launch_bounds__(512, 2) void k2_attn(const u16* __restrict__ x, const u16* __restrict__ b2, char* __restrict__ ws,
                                                  u16* __restrict__ out) {
    __shared__ __attribute__((aligned(1024))) char smem[K2_LDS];
    lds_ptr lds = (lds_ptr)smem;
    const unsigned lds0 = (unsigned)(uintptr_t)lds;
    const int tid = threadIdx.x, lane = tid & 63, r32 = lane & 31, hi = lane >> 5;
    const int w = __builtin_amdgcn_readfirstlane(tid >> 6);
    const int b = blockIdx.x, xcd = b & 7, idx = b >> 3;
    const int h = xcd * 4 + (idx >> 3), qb = idx & 7;
    const int odd = h & 1, plane0 = 5 * (h >> 1) + 2 * odd;
    const int mp = odd, xp = odd ? 0 : 2;
    const int tok = qb * 256 + w * 32 + r32;
    VSTAMP(0);
    u32x4 q0, q1;
    {
        const char* qp0 = ws + WS_QK + ((size_t)(plane0 + mp + hi) * SEQ + tok) * 16;
        const char* qp1 = ws + WS_QK + ((size_t)(plane0 + xp) * SEQ + tok) * 16;
        asm volatile("global_load_dwordx4 %0, %1, off" : "=v"(q0) : "v"(qp0) : "memory");
        asm volatile("global_load_dwordx4 %0, %1, off" : "=v"(q1) : "v"(qp1) : "memory");
    }
    unsigned doff[3], dstr[3];
    unsigned long long dmask[3];
#pragma unroll
    for (int i = 0; i < 3; ++i) {
        const int n = w + 8 * i;
        const int gl = lane < 63 ? lane / 21 : 0, d = lane < 63 ? lane - gl * 21 : 0;
        doff[i] = n < 12 ? (unsigned)WS_QK + ((80 + plane0 + (n >> 2)) * SEQ + (n & 3) * 64 + lane) * 16
                         : (unsigned)WS_V + ((3 * (n - 12) + gl) * DM + 20 * h + d) * 16;
        dstr[i] = n < 12 ? 256 * 16 : 32 * DM * 16;
        dmask[i] = n < 12 ? ~0ull : n < 22 ? ~((1ull << 20) | (1ull << 41) | (3ull << 62)) : ((1ull << 41) - 1) & ~(1ull << 20);
    }
    const int kap = (r32 & 19) | ((r32 & 4) << 1) | ((r32 & 8) >> 1);
    const int mb = qb * 4 + (xcd & 3), nb = (xcd >> 2) * 4 + (idx >> 3), th = w & 1;
    const __amdgpu_buffer_rsrc_t rs = __builtin_amdgcn_make_buffer_rsrc(ws, 0, (unsigned)WS_END, 0x00020000);
    const unsigned hoff = ((lane >> 4) * SEQ + (lane & 15)) * 16, woff = ((lane >> 4) * DM + (lane & 15)) * 16;
    u32x4 fr[54];
    unsigned hbase, wbase;
    auto ldfrag = [&](auto F_) {
        constexpr int F = decltype(F_)::v, ks = F / 9, i = F % 9;
        if constexpr (i < 5) fr[F] = __builtin_amdgcn_raw_buffer_load_b128(rs, woff, wbase + ks * 4 * DM * 16 + i * 256, 0);
        else fr[F] = __builtin_amdgcn_raw_buffer_load_b128(rs, hoff, (hbase ^ (((i - 5) >> 1) * 512)) + ks * 4 * SEQ * 16 + ((i - 5) & 1) * 256, 0);
    };
    unsigned km = K2_RING + K2_SLOT + (mp + hi) * 4096 + kap * 16, kx = K2_RING + K2_SLOT + xp * 4096 + kap * 16, vb[3];
#pragma unroll
    for (int r = 0; r < 3; ++r) vb[r] = K2_RING + K2_SLOT + K2_VOFF + (r32 > 20 ? 1008 - 336 * r : r32 * 16 + hi * (r == 2 ? 352 : 336));
    f32x4 acc[4][5];
    auto kstep = [&](auto KS_, auto&& act) {
        constexpr int ks = decltype(KS_)::v;
        sfor<0, 20>([&](auto J_) {
            constexpr int j = decltype(J_)::v, m = j / 5, i = j % 5;
            acc[m][i] = mfma16(fr[9 * ks + i], fr[9 * ks + 5 + m], ks ? acc[m][i] : f32x4{0.f, 0.f, 0.f, 0.f});
            act(J_);
            SBAR();
        });
    };
    gu32* cnt = (gu32*)(ws + WS_CNT + qb * 256);
    float mref = 0.f;
    const f32x16 zero = {0.f, 0.f, 0.f, 0.f, 0.f, 0.f, 0.f, 0.f, 0.f, 0.f, 0.f, 0.f, 0.f, 0.f, 0.f, 0.f};
    for (int att = 0;; ++att) {
        unsigned lw = lds0 + w * 1024;
        hbase = (unsigned)WS_H + (24 * w * SEQ + mb * 64 + th * 32) * 16;
        wbase = (unsigned)WS_WT + ((80 + 24 * w) * DM + nb * 80) * 16;
        asm volatile("" : "+s"(lw), "+s"(hbase), "+s"(wbase));
        auto piece = [&](int i, unsigned slot) {
            if (i < 2 || w < 7) glds16m(doff[i], ws, lw + K2_RING + slot + 8 * i * 1024, dmask[i]);
            doff[i] += dstr[i];
            PIN(doff[i]);
        };
        auto wopiece = [&](int r) {
            const unsigned p = 8 * r + w, ks = (p * 205u) >> 10, i = p - 5 * ks;
            glds16(woff, ws + WS_WT + (ks * 4 * DM + nb * 80 + i * 16) * 16, lw + r * 8192);
        };
#pragma unroll
        for (int c = 0; c < 2; ++c)
#pragma unroll
            for (int i = 0; i < 3; ++i) piece(i, (c + 1) * K2_SLOT);
        if (att == 0 && tid < 132) {
            const int sl = tid / 44, r = tid - sl * 44;
            unsigned one = (r & 3) == 3 ? 0u : ONES2;
            PIN(one);
            *(__attribute__((address_space(3))) u32x4*)(lds + K2_RING + sl * K2_SLOT + K2_VOFF + (r >> 2) * 1024 + ((r & 3) == 3 ? 1008 : 320 + (r & 3) * 336)) = u32x4{one, one, one, one};
        }
        if (tid == 0) *(__attribute__((address_space(3))) u32x2*)(lds + K2_FLAG) = u32x2{0u, 0u};
        asm volatile("s_waitcnt vmcnt(2) lgkmcnt(0)\n\ts_barrier" : "+v"(q0), "+v"(q1) :: "memory");
        VSTAMP(1);
        __builtin_amdgcn_s_setprio(3);
        if (hi) q1 = u32x4{0u, 0u, 0u, 0u};
        else if (odd) { q1[0] = 0u; q1[1] = 0u; }
        else { q1[2] = 0u; q1[3] = 0u; }
        u32x4 kf0 = lds_rd128(lds + km), kf1 = lds_rd128(lds + kx), vf0, vf1, p0, p1;
        f32x16 s[2], negm, o;
        s[0] = mfma32(kf0, q0, zero);
        s[0] = mfma32(kf1, q1, s[0]);
        kf0 = lds_rd128(lds + km + 512);
        kf1 = lds_rd128(lds + kx + 512);
        vf0 = lds_rd128(lds + vb[0]);
        {
            unsigned z = 0u;
            PIN(z);
            vf1 = p1 = u32x4{z, z, z, z};
#pragma unroll
            for (int r = 0; r < 16; ++r) o[r] = __uint_as_float(z);
        }
        if (att == 0) {
            float m = s[0][0];
#pragma unroll
            for (int r = 1; r < 16; ++r) m = __builtin_fmaxf(m, s[0][r]);
            auto rr = __builtin_amdgcn_permlane32_swap(__float_as_uint(m), __float_as_uint(m), false, false);
            mref = __builtin_fmaxf(__uint_as_float(rr[0]), __uint_as_float(rr[1]));
        }
#pragma unroll
        for (int r = 0; r < 16; ++r) { negm[r] = -mref; s[0][r] -= mref; }
        PIN(negm);
        SBAR();
        auto tile = [&](auto TT_, auto KIND_, int c, unsigned step, unsigned nslot) {
            constexpr int TT = decltype(TT_)::v, KIND = decltype(KIND_)::v, CU = TT & 1, NX = CU ^ 1;
            auto extra = [&](auto G_) {
                constexpr int G = decltype(G_)::v;
                if constexpr (KIND == 0) {
                    if constexpr (G == 1 && TT == 1) wopiece(2 * c);
                    if constexpr (G == 1 && TT == 3) { if (c < 5) wopiece(2 * c + 1); }
                    if constexpr (TT >= 6 && (G & 1) && (TT - 6) * 2 + (G >> 1) < 3) piece((TT - 6) * 2 + (G >> 1), nslot);
                } else {
                    if constexpr (!(G & 1)) ldfrag(IC<(KIND - 1) * 16 + TT * 2 + (G >> 1)>{});
                    if constexpr (KIND == 1 && G == 1 && TT == 6) wopiece(11);
                    if constexpr (KIND == 1 && G == 1 && TT == 7) { if (w < 4) wopiece(12); }
                }
                SBAR();
            };
            auto soft = [&](auto G_) {
                constexpr int G = decltype(G_)::v;
                u32x4& p = G < 2 ? p0 : p1;
                float e0 = __builtin_amdgcn_exp2f(s[CU][4 * G]), e1 = __builtin_amdgcn_exp2f(s[CU][4 * G + 1]);
                PIN(e0); PIN(e1);
                SBAR();
                extra(G_);
                float e2 = __builtin_amdgcn_exp2f(s[CU][4 * G + 2]), e3 = __builtin_amdgcn_exp2f(s[CU][4 * G + 3]);
                PIN(e2); PIN(e3);
                SBAR();
                p[2 * (G & 1)] = pk2(e0, e1);
                p[2 * (G & 1) + 1] = pk2(e2, e3);
                PIN(p);
                SBAR();
            };
            if constexpr (TT == 6) {
                if constexpr (KIND == 0) asm volatile("s_waitcnt vmcnt(1)\n\ts_barrier" ::: "memory");
                else if constexpr (KIND == 1) asm volatile("s_waitcnt vmcnt(12)\n\ts_barrier" ::: "memory");
                km += step;
                kx += step;
                SBAR();
            }
            s[NX] = mfma32(kf0, q0, negm);
            kf0 = lds_rd128(lds + km + ((TT + 2) & 7) * 512);
            SBAR();
            soft(IC<0>{});
            s[NX] = mfma32(kf1, q1, s[NX]);
            kf1 = lds_rd128(lds + kx + ((TT + 2) & 7) * 512);
            SBAR();
            soft(IC<1>{});
            o = mfma32(vf1, p1, o);
            vf1 = lds_rd128(lds + vb[(4 * TT + 2) % 3] + ((4 * TT + 2) / 3) * 1024 + ((4 * TT + 2) % 3) * 336);
            SBAR();
            soft(IC<2>{});
            o = mfma32(vf0, p0, o);
            if constexpr (TT == 7) {
#pragma unroll
                for (int r = 0; r < 3; ++r) vb[r] += step;
            }
            constexpr int GN = (4 * TT + 4) & 31;
            vf0 = lds_rd128(lds + vb[GN % 3] + (GN / 3) * 1024 + (GN % 3) * 336);
            SBAR();
            soft(IC<3>{});
        };
        auto body = [&](auto KIND_, int c, unsigned step, unsigned nslot) {
            tile(IC<0>{}, KIND_, c, step, nslot); tile(IC<1>{}, KIND_, c, step, nslot); tile(IC<2>{}, KIND_, c, step, nslot);
            tile(IC<3>{}, KIND_, c, step, nslot); tile(IC<4>{}, KIND_, c, step, nslot); tile(IC<5>{}, KIND_, c, step, nslot);
            tile(IC<6>{}, KIND_, c, step, nslot); tile(IC<7>{}, KIND_, c, step, nslot);
        };
        unsigned nslot = 0;
#pragma unroll 1
        for (int c = 0; c < 6; ++c) {
            body(IC<0>{}, c, nslot == (unsigned)K2_SLOT ? (unsigned)(-2 * K2_SLOT) : (unsigned)K2_SLOT, nslot);
            nslot = nslot == 2u * K2_SLOT ? 0u : nslot + K2_SLOT;
            VSTAMP(2 + c);
        }
        body(IC<1>{}, 6, K2_SLOT, 0);
        VSTAMP(8);
        body(IC<2>{}, 7, -K2_SLOT, 0);
        VSTAMP(9);
        o = mfma32(vf1, p1, o);
        {
            auto rr = __builtin_amdgcn_permlane32_swap(__float_as_uint(o[8]), __float_as_uint(o[8]), false, false);
            const float l = __uint_as_float(rr[1]);
            const float inv = 1.0f / l;
            int ln = lane;
            PIN(ln);
            const unsigned ooff = (ln & 31) * 16 + (odd ? (ln >> 5 ? SEQ * 16 : 8) : (ln >> 5) * 8);
#pragma unroll
            for (int qd = 0; qd < 3; ++qd) {
                u32x2 v = {pk2(o[4 * qd] * inv, o[4 * qd + 1] * inv), pk2(o[4 * qd + 2] * inv, o[4 * qd + 3] * inv)};
                if (qd < 2 || ln < 32) __builtin_amdgcn_raw_buffer_store_b64(v, rs, ooff, (unsigned)WS_O + ((plane0 + qd) * SEQ + qb * 256 + w * 32) * 16, 16);
            }
            SBAR();
            bool ok = l <= 1.2676506e30f;
#pragma unroll
            for (int r = 0; r < 12; ++r) ok = ok && (__builtin_fabsf(o[r]) < __builtin_inff());
            if (!ok) *(__attribute__((address_space(3))) unsigned*)(lds + K2_FLAG) = 1u;
        }
        SBAR();
        VSTAMP(18);
        __builtin_amdgcn_s_setprio(0);
        kstep(IC<0>{}, [&](auto) {});
        VSTAMP(19);
        __builtin_amdgcn_s_setprio(3);
        asm volatile("s_waitcnt vmcnt(0) lgkmcnt(0)" ::: "memory");
        unsigned arr = 0u;
        if (lane == 0) arr = __hip_atomic_fetch_add((__attribute__((address_space(3))) unsigned*)(lds + K2_FLAG + 4), 1u, __ATOMIC_RELAXED, __HIP_MEMORY_SCOPE_WORKGROUP);
        bool sig = __builtin_amdgcn_readfirstlane(arr) == 7u;
        if (sig) {
            asm volatile("" ::: "memory");
            sig = att || !__builtin_amdgcn_readfirstlane(*(volatile __attribute__((address_space(3))) unsigned*)(lds + K2_FLAG));
            if (sig && lane == 0) __hip_atomic_fetch_add(cnt, 1u, __ATOMIC_RELAXED, __HIP_MEMORY_SCOPE_AGENT);
        }
        __builtin_amdgcn_s_setprio(0);
        SBAR();
        VSTAMP(10);
        kstep(IC<1>{}, [&](auto J_) { constexpr int j = decltype(J_)::v; if constexpr (j % 2 == 0) ldfrag(IC<32 + j / 2>{}); });
        VSTAMP(21);
        kstep(IC<2>{}, [&](auto J_) { constexpr int j = decltype(J_)::v; if constexpr (j % 3 == 0) ldfrag(IC<42 + j / 3>{}); });
        VSTAMP(22);
        kstep(IC<3>{}, [&](auto J_) { constexpr int j = decltype(J_)::v; if constexpr (j % 3 == 0 && j < 15) ldfrag(IC<49 + j / 3>{}); });
        VSTAMP(11);
        if (sig) {
            for (unsigned spins = 0; spins < (1u << 17); ++spins) {
                if ((unsigned)__builtin_amdgcn_readfirstlane(__hip_atomic_load(cnt, __ATOMIC_RELAXED, __HIP_MEMORY_SCOPE_AGENT)) >= 32u) break;
                __builtin_amdgcn_s_sleep(1);
            }
        }
        asm volatile("s_barrier" ::: "memory");
        if (att) break;
        const unsigned fl = __builtin_amdgcn_readfirstlane(*(__attribute__((address_space(3))) unsigned*)(lds + K2_FLAG));
        if (!fl) break;
        {
#pragma unroll
            for (int i = 0; i < 3; ++i) doff[i] -= 8 * dstr[i];
            float mx = -__builtin_inff();
            int ln = lane;
            PIN(ln);
#pragma unroll 1
            for (int t = 0; t < 64; ++t) {
                const u32x4 ka = __builtin_amdgcn_raw_buffer_load_b128(rs, ((ln >> 5) * SEQ + (ln & 31)) * 16, (unsigned)WS_QK + (80 + plane0 + mp) * SEQ * 16 + t * 512, 0);
                const u32x4 kb = __builtin_amdgcn_raw_buffer_load_b128(rs, (ln & 31) * 16, (unsigned)WS_QK + (80 + plane0 + xp) * SEQ * 16 + t * 512, 0);
                f32x16 z = mfma32(ka, q0, zero);
                z = mfma32(kb, q1, z);
#pragma unroll
                for (int r = 0; r < 16; ++r) mx = __builtin_fmaxf(mx, z[r]);
            }
            auto rr = __builtin_amdgcn_permlane32_swap(__float_as_uint(mx), __float_as_uint(mx), false, false);
            mref = __builtin_fmaxf(__uint_as_float(rr[0]), __uint_as_float(rr[1]));
        }
        WAIT_BAR(0);
    }
    int ln = lane;
    PIN(ln);
    VSTAMP(12);
    u32x4 of[5][2], wq[5][5];
    const unsigned obase = (unsigned)WS_O + (20 * (w >> 1) * SEQ + mb * 64 + th * 32) * 16;
    const unsigned wqb = (w >> 1) * 25600 + ln * 16;
    kstep(IC<4>{}, [&](auto J_) {
        constexpr int j = decltype(J_)::v;
        if constexpr (j % 2 == 0) of[(j / 2) % 5][j / 10] = __builtin_amdgcn_raw_buffer_load_b128(rs, hoff, obase + ((j / 2) % 5) * 4 * SEQ * 16 + (j / 10) * 256, 16);
        else if constexpr (j < 16) wq[j / 10][(j / 2) % 5] = lds_rd128(lds + wqb + (j / 2) * 1024);
    });
    VSTAMP(23);
    kstep(IC<5>{}, [&](auto J_) {
        constexpr int j = decltype(J_)::v;
        if constexpr (j < 9) wq[(8 + j) / 5][(8 + j) % 5] = lds_rd128(lds + wqb + (8 + j) * 1024);
    });
    sfor<17, 25>([&](auto J_) { constexpr int j = decltype(J_)::v; wq[j / 5][j % 5] = lds_rd128(lds + wqb + j * 1024); });
    asm volatile("s_waitcnt lgkmcnt(0)\n\ts_barrier" ::: "memory");
    VSTAMP(13);
    unsigned eoff[3];
    u32x2 xv[3], bv[3];
#pragma unroll
    for (int r = 0; r < 3; ++r) {
        const unsigned u = 160 * w + 64 * r + (r < 2 ? ln : ln & 31), sub = u >> 6, m = (sub * 205u) >> 10, i = sub - 5 * m;
        const unsigned col = nb * 80 + i * 16 + ((u >> 4) & 3) * 4;
        eoff[r] = ((mb * 64 + m * 16 + (u & 15)) * DM + col) * 2;
        xv[r] = *(const u32x2*)((const char*)x + eoff[r]);
        bv[r] = *(const u32x2*)((const char*)b2 + col * 2);
    }
    SBAR();
    const unsigned rw = w * 20480 + ln * 16, rwa = rw + (th ^ 1) * 10240, rwb = rw + th * 10240;
    sfor<0, 25>([&](auto J_) {
        constexpr int j = decltype(J_)::v;
        if constexpr (j % 2 == 0 && j < 20) lds_wrf4(lds + rwa + (j / 2) * 1024, acc[2 + j / 10][(j / 2) % 5]);
        acc[0][j % 5] = mfma16(wq[j / 5][j % 5], of[j / 5][0], acc[0][j % 5]);
        SBAR();
    });
    sfor<0, 25>([&](auto J_) {
        constexpr int j = decltype(J_)::v, i = j < 10 ? j % 2 : 2 + (j - 10) % 3, k = j < 10 ? j / 2 : (j - 10) / 3;
        acc[1][i] = mfma16(wq[k][i], of[k][1], acc[1][i]);
        if constexpr (j % 2 == 1 && j < 10) lds_wrf4(lds + rwb + (j / 2) * 1024, acc[0][j / 2]);
        if constexpr (j == 13 || j == 16) lds_wrf4(lds + rwb + (5 + (j - 13) / 3) * 1024, acc[1][(j - 13) / 3]);
        SBAR();
    });
    VSTAMP(14);
#pragma unroll
    for (int i = 2; i < 5; ++i) lds_wrf4(lds + rwb + (5 + i) * 1024, acc[1][i]);
    asm volatile("s_waitcnt lgkmcnt(0)\n\ts_barrier" ::: "memory");
    VSTAMP(15);
#pragma unroll
    for (int r = 0; r < 3; ++r) {
        lds_ptr p = lds + (160 * w + 64 * r + (r < 2 ? ln : ln & 31)) * 16;
        f32x4 t[8];
#pragma unroll
        for (int k = 0; k < 8; ++k) t[k] = lds_rdf4(p + k * 20480);
        const f32x4 s = ((t[0] + t[1]) + (t[2] + t[3])) + ((t[4] + t[5]) + (t[6] + t[7]));
        u32x2 v;
        v[0] = pk2(s[0] + bf_lo(xv[r][0]) + bf_lo(bv[r][0]), s[1] + bf_hi(xv[r][0]) + bf_hi(bv[r][0]));
        v[1] = pk2(s[2] + bf_lo(xv[r][1]) + bf_lo(bv[r][1]), s[3] + bf_hi(xv[r][1]) + bf_hi(bv[r][1]));
        if (r < 2 || ln < 32) *(u32x2*)((char*)out + eoff[r]) = v;
    }
}

extern "C" void kernel_launch(void* const* d_in, const int* in_sizes, int n_in, void* d_out, int out_size, void* d_ws, size_t ws_size,
                              hipStream_t stream) {
    const u16* x = (const u16*)d_in[0];
    const u16* wqkv = (const u16*)d_in[1];
    const u16* wo = (const u16*)d_in[2];
    const u16* w1 = (const u16*)d_in[4];
    const u16* b1 = (const u16*)d_in[5];
    const u16* w2 = (const u16*)d_in[6];
    const u16* b2 = (const u16*)d_in[7];
    char* ws = (char*)d_ws;
    k1_qkv_mlp1<<<256, 512, 0, stream>>>(x, wqkv, w1, b1, wo, w2, ws);
    k2_attn<<<256, 512, 0, stream>>>(x, b2, ws, (u16*)d_out);
}
```

```cpp
#include <hip/hip_runtime.h>
#include <stdint.h>

typedef __attribute__((ext_vector_type(8))) __bf16 bf16x8;
typedef __attribute__((ext_vector_type(2))) __bf16 bf16x2;
typedef __attribute__((ext_vector_type(4))) float f32x4;
typedef __attribute__((ext_vector_type(2))) float f32x2;
typedef __attribute__((ext_vector_type(16))) float f32x16;
typedef __attribute__((ext_vector_type(4))) unsigned u32x4;
typedef __attribute__((ext_vector_type(2))) unsigned u32x2;
typedef short v4i16_t __attribute__((ext_vector_type(4)));
typedef __attribute__((address_space(3))) char* lds_ptr;
typedef unsigned short u16;
template <int V> struct IC { static constexpr int v = V; };

#define SBAR() __builtin_amdgcn_sched_barrier(0)
#define PIN(x) asm volatile("" : "+v"(x))
#define WAIT_BAR(N) asm volatile("s_waitcnt vmcnt(" #N ") lgkmcnt(0)\n\ts_barrier" ::: "memory")
#define WAIT_VM(N) asm volatile("s_waitcnt vmcnt(" #N ")" ::: "memory")
#define LGKM_BAR() asm volatile("s_waitcnt lgkmcnt(0)\n\ts_barrier" ::: "memory")

#define PROF_LDS 0
#define STAMP(lds, base, i) do {} while (0)
#define STAMP_DUMP(ws, lds, base, kid, n) do {} while (0)
constexpr int SEQ = 2048, DM = 640, FF = 1536;
constexpr size_t WS_QK = 0;
constexpr size_t WS_V = WS_QK + (size_t)160 * SEQ * 16;
constexpr size_t WS_O = WS_V + (size_t)256 * DM * 16;
constexpr size_t WS_H = WS_O + (size_t)80 * SEQ * 16;
constexpr size_t WS_WT = WS_H + (size_t)192 * SEQ * 16;
constexpr size_t WS_P = WS_WT + (size_t)272 * DM * 16;
constexpr size_t WS_CNT = WS_P;
constexpr size_t WS_END = WS_P + (size_t)160 * SEQ * 16;

__device__ __forceinline__ void glds16(unsigned voff, const void* sbase, unsigned lds_base) {
    unsigned sv;
    asm volatile("s_mov_b32 %0, m0\n\ts_mov_b32 m0, %3\n\ts_nop 0\n\tglobal_load_lds_dwordx4 %1, %2\n\ts_mov_b32 m0, %0"
                 : "=&s"(sv) : "v"(voff), "s"(sbase), "s"(lds_base) : "memory");
}
__device__ __forceinline__ u32x2 tr_read(lds_ptr p) {
    return __builtin_bit_cast(u32x2, __builtin_amdgcn_ds_read_tr16_b64_v4i16((__attribute__((address_space(3))) v4i16_t*)p));
}
__device__ __forceinline__ u32x4 lds_rd128(lds_ptr p) { return *(__attribute__((address_space(3))) u32x4*)p; }
__device__ __forceinline__ f32x4 lds_rdf4(lds_ptr p) { return *(__attribute__((address_space(3))) f32x4*)p; }
__device__ __forceinline__ f32x2 lds_rdf2(lds_ptr p) { return *(__attribute__((address_space(3))) f32x2*)p; }
__device__ __forceinline__ float lds_rdf(lds_ptr p) { return *(__attribute__((address_space(3))) float*)p; }
__device__ __forceinline__ void lds_wrf4(lds_ptr p, f32x4 v) { *(__attribute__((address_space(3))) f32x4*)p = v; }
__device__ __forceinline__ void lds_wrf2(lds_ptr p, f32x2 v) { *(__attribute__((address_space(3))) f32x2*)p = v; }
__device__ __forceinline__ void lds_wrf(lds_ptr p, float v) { *(__attribute__((address_space(3))) float*)p = v; }
__device__ __forceinline__ f32x4 mfma16(u32x4 a, u32x4 b, f32x4 c) {
    return __builtin_amdgcn_mfma_f32_16x16x32_bf16(__builtin_bit_cast(bf16x8, a), __builtin_bit_cast(bf16x8, b), c, 0, 0, 0);
}
__device__ __forceinline__ f32x16 mfma32(u32x4 a, u32x4 b, f32x16 c) {
    return __builtin_amdgcn_mfma_f32_32x32x16_bf16(__builtin_bit_cast(bf16x8, a), __builtin_bit_cast(bf16x8, b), c, 0, 0, 0);
}
__device__ __forceinline__ unsigned pk2(float lo, float hi) {
    bf16x2 v = {(__bf16)lo, (__bf16)hi};
    return __builtin_bit_cast(unsigned, v);
}
__device__ __forceinline__ float bf_lo(unsigned u) { return __uint_as_float(u << 16); }
__device__ __forceinline__ float bf_hi(unsigned u) { return __uint_as_float(u & 0xffff0000u); }
constexpr unsigned ONES2 = 0x3f803f80u;

constexpr int K1_STAGE = 45056, K1_QOFF = 16384, K1_MOFF = 32768;
constexpr int K1_ROWST = 3 * K1_STAGE;
constexpr int K1_COLS = K1_ROWST + 2048;
constexpr int K1_TR = K1_COLS + 2048;
constexpr int K1_LDS = K1_TR + 16 * 1024;

__device__ __forceinline__ f32x2 gelu_tanh2(f32x2 u) {
    const float c1 = -2.0f * 0.7978845608028654f * 1.4426950408889634f;
    const float c2 = c1 * 0.044715f;
    f32x2 t = u * __builtin_elementwise_fma(u * u, f32x2{c2, c2}, f32x2{c1, c1});
    f32x2 d = f32x2{__builtin_amdgcn_exp2f(t[0]), __builtin_amdgcn_exp2f(t[1])} + f32x2{1.0f, 1.0f};
    return u * f32x2{__builtin_amdgcn_rcpf(d[0]), __builtin_amdgcn_rcpf(d[1])};
}

struct K1Dma { unsigned vx, vq, vm0, vm1, tv; const char *xg, *qg, *mg, *tsrc0, *tsrc1; char *tdst0, *tdst1, *zdst; unsigned dst; bool t1; };

template <int TYPE, int WM>
__device__ __forceinline__ void k1_loop(lds_ptr lds, const K1Dma& dm, const unsigned (&xa)[2][2], const unsigned (&wa)[2][4],
                                        f32x4 (&acc)[4][4], f32x4 (&cs)[4], f32x4 (&st)[4], const u16* bp, u32x2 (&bb)[3]) {
    constexpr int NT = TYPE == 2 ? 3 : 4;
    constexpr int HI = TYPE == 2 ? 768 : 512;
    constexpr int NR = 4 + 2 * NT;
    const u32x4 ones = {ONES2, ONES2, ONES2, ONES2};
    u32x4 xf[2][4];
    u32x2 wl[2][4], wh[2][4];
    auto piece = [&](int j, int s, unsigned so) {
        const unsigned d = dm.dst + so + j * 8192;
        if (j == 0) glds16(dm.vx, dm.xg + s * 128, d);
        if (j == 1) glds16(dm.vx, dm.xg + s * 128 + 64 * DM * 2, d);
        if (j == 2) glds16(dm.vq, dm.qg + (size_t)s * (64 * 3 * DM * 2), d);
        if (j == 3) glds16(dm.vq, dm.qg + (size_t)s * (64 * 3 * DM * 2) + 32 * 3 * DM * 2, d);
        if (j == 4) glds16(dm.vm0, dm.mg + (size_t)s * (64 * FF * 2), d);
        if (j == 5 && WM == 0) glds16(dm.vm1, dm.mg + (size_t)s * (64 * FF * 2), d);
    };
    auto rd = [&](auto F_, int n, unsigned so) {
        constexpr int F = decltype(F_)::v;
        const int k = n == 0 ? 0 : n < 3 ? n + 3 : n < 6 ? n - 2 : n;
        if (k < 4) xf[F][k] = lds_rd128(lds + so + xa[F][k >> 1] + (k & 1) * 2048);
        else if (((k - 4) & 1) == 0) wl[F][(k - 4) >> 1] = tr_read(lds + so + wa[F][(k - 4) >> 1]);
        else wh[F][(k - 4) >> 1] = tr_read(lds + so + wa[F][(k - 4) >> 1] + HI);
    };
    auto mm = [&](auto F_, int n) {
        constexpr int F = decltype(F_)::v;
        if (n < 4 * NT) {
            const int i = n >> 2, m = n & 3;
            const u32x4 wf = {wl[F][i][0], wl[F][i][1], wh[F][i][0], wh[F][i][1]};
            acc[m][i] = TYPE == 1 ? mfma16(xf[F][m], wf, acc[m][i]) : mfma16(wf, xf[F][m], acc[m][i]);
        } else if (TYPE == 2 && n < 16) {
            const int m = n & 1;
            if (n < 14) st[m] = mfma16(ones, xf[F][m], st[m]);
            else st[2 + m] = mfma16(xf[F][m], xf[F][m], st[2 + m]);
        } else {
            const int i = n - 16;
            const u32x4 wf = {wl[F][i][0], wl[F][i][1], wh[F][i][0], wh[F][i][1]};
            cs[i] = TYPE == 1 ? mfma16(ones, wf, cs[i]) : mfma16(wf, ones, cs[i]);
        }
    };
    auto phase = [&](auto F_, auto RD_, auto DMA_, unsigned rso, int ds, unsigned dso) {
        constexpr int F = decltype(F_)::v;
        constexpr int NM = F == 0 ? 16 + NT : 16;
#pragma unroll
        for (int n = 0; n < NM; ++n) {
            mm(F_, n);
            if (decltype(RD_)::v && n < NR) rd(IC<1 - F>{}, n, rso);
            SBAR();
            if (decltype(DMA_)::v && n % 3 == 0 && n / 3 < 6) { piece(n / 3, ds, dso); SBAR(); }
        }
    };
    STAMP(lds, K1_LDS, 15);
    if (WM == 0) { WAIT_BAR(6); } else { WAIT_BAR(5); }
    STAMP(lds, K1_LDS, 1);
#pragma unroll
    for (int n = 0; n < NR; ++n) rd(IC<0>{}, n, 0);
    phase(IC<0>{}, IC<1>{}, IC<1>{}, 0, 2, 2 * K1_STAGE);
    if (WM == 0) { WAIT_BAR(6); } else { WAIT_BAR(5); }
    unsigned so = 0;
    for (int s = 0; s < 7; ++s) {
        STAMP(lds, K1_LDS, 2 + s);
        const unsigned sn = so == 2 * K1_STAGE ? 0 : so + K1_STAGE;
        phase(IC<1>{}, IC<1>{}, IC<WM == 0>{}, sn, s + 3, so);
        phase(IC<0>{}, IC<1>{}, IC<WM == 1>{}, sn, s + 3, so);
        if (WM == 0) { WAIT_BAR(6); } else { WAIT_BAR(5); }
        so = sn;
    }
    for (int s = 7; s < 9; ++s) {
        STAMP(lds, K1_LDS, 2 + s);
        const unsigned sn = so == 2 * K1_STAGE ? 0 : so + K1_STAGE;
        phase(IC<1>{}, IC<1>{}, IC<0>{}, sn, 0, 0);
        phase(IC<0>{}, IC<1>{}, IC<0>{}, sn, 0, 0);
        if (s == 7) {
            WAIT_BAR(0);
            glds16(dm.tv, dm.tsrc0, dm.dst + K1_TR);
            glds16(dm.tv, dm.tsrc1, dm.dst + K1_TR + 8192);
            *(u32x4*)(dm.zdst + threadIdx.x * 16) = u32x4{0u, 0u, 0u, 0u};
            if (threadIdx.x < 128) *(u32x4*)(dm.zdst + 8192 + threadIdx.x * 16) = u32x4{0u, 0u, 0u, 0u};
            if constexpr (TYPE == 2) {
                asm volatile("global_load_dwordx2 %0, %3, off\n\tglobal_load_dwordx2 %1, %3, off offset:32\n\tglobal_load_dwordx2 %2, %3, off offset:64"
                             : "=&v"(bb[0]), "=&v"(bb[1]), "=&v"(bb[2]) : "v"(bp) : "memory");
            }
        }
        so = sn;
    }
    STAMP(lds, K1_LDS, 11);
    phase(IC<1>{}, IC<0>{}, IC<0>{}, 0, 0, 0);
    if constexpr (TYPE == 2) asm volatile("s_waitcnt vmcnt(0)" : "+v"(bb[0]), "+v"(bb[1]), "+v"(bb[2]) :: "memory");
}

template <int TYPE>
__device__ __forceinline__ void k1_body(lds_ptr lds, const K1Dma& dm, int wm, int wn, int T0, const u16* x, const u16* b1, char* ws,
                                        int mb, int lane, bool live) {
    constexpr int NT = TYPE == 2 ? 3 : 4;
    const int g = lane >> 4, l15 = lane & 15;
    const int pm = TYPE == 2 ? 2 * (wn & 1) : 0;
    u32x2 bb[3];
    const u16* bp = b1 + T0 * 16 + g * 4;
    unsigned xa[2][2], wa[2][4];
    {
        const int t = wm * 64 + l15, f = (t >> 1) & 7;
        const int q = (lane >> 2) & 3, p = lane & 3;
#pragma unroll
        for (int F = 0; F < 2; ++F) {
            const int ks = wm ^ F;
            const unsigned xb = t * 128 + (((4 * ks + g) ^ f) * 16);
            xa[F][0] = xb + pm * 2048;
            xa[F][1] = xb + (2 - pm) * 2048;
#pragma unroll
            for (int a = 0; a < 4; ++a) {
                if (TYPE == 2) wa[F][a] = K1_MOFF + (ks * 4 + g) * 1536 + q * 192 + (((3 * (wn & 1) + a) ^ (g & 1)) * 32) + p * 8;
                else wa[F][a] = K1_QOFF + (ks * 4 + g) * 2048 + wn * 1024 + q * 128 + ((a ^ (((q >> 1) & 1) | ((g & 1) << 1))) * 32) + p * 8;
            }
        }
    }
    f32x4 acc[4][4], cs[4], st[4];
#pragma unroll
    for (int i = 0; i < 4; ++i) {
        cs[i] = f32x4{0.f, 0.f, 0.f, 0.f};
        st[i] = f32x4{0.f, 0.f, 0.f, 0.f};
#pragma unroll
        for (int m = 0; m < 4; ++m) acc[m][i] = f32x4{0.f, 0.f, 0.f, 0.f};
    }
    if (wm == 0) k1_loop<TYPE, 0>(lds, dm, xa, wa, acc, cs, st, bp, bb);
    else k1_loop<TYPE, 1>(lds, dm, xa, wa, acc, cs, st, bp, bb);
    STAMP(lds, K1_LDS, 12);
    {
        WAIT_VM(0);
        lds_ptr tp = (lds_ptr)(uintptr_t)dm.dst + K1_TR + ((lane >> 2) & 3) * 128 + g * 32 + (lane & 3) * 8;
        u32x2 lo = tr_read(tp), hi = tr_read(tp + 512);
        u32x2 lo1 = tr_read(tp + 8192), hi1 = tr_read(tp + 8192 + 512);
        *(u32x4*)(dm.tdst0 + lane * 16) = u32x4{lo[0], lo[1], hi[0], hi[1]};
        if (dm.t1) *(u32x4*)(dm.tdst1 + lane * 16) = u32x4{lo1[0], lo1[1], hi1[0], hi1[1]};
    }
    if constexpr (TYPE == 2) {
        if ((l15 >> 2) == g) {
#pragma unroll
            for (int m = 0; m < 2; ++m) {
                const int r = l15 & 3;
                const float rq = r == 0 ? st[2 + m][0] : r == 1 ? st[2 + m][1] : r == 2 ? st[2 + m][2] : st[2 + m][3];
                const int tl = wm * 64 + (pm + m) * 16 + l15;
                const float mu = st[m][0] * (1.0f / DM);
                float var = __builtin_fmaf(-mu, mu, rq * (1.0f / DM));
                if (!(var >= mu * mu * (1.0f / 256.0f))) {
                    const u16* xr = x + (size_t)(mb * 128 + tl) * DM;
                    float a = 0.f;
                    for (int k = 0; k < DM; ++k) {
                        float d = __uint_as_float((unsigned)xr[k] << 16) - mu;
                        a = __builtin_fmaf(d, d, a);
                    }
                    var = a * (1.0f / DM);
                }
                const float r1 = __builtin_amdgcn_rsqf(var + 1e-5f);
                const float r2 = r1 * __builtin_amdgcn_rsqf(__builtin_fmaf(var * r1, r1, 1e-5f));
                lds_wrf2(lds + K1_ROWST + tl * 8, f32x2{-mu * r1, r1});
                lds_wrf2(lds + K1_ROWST + 1024 + tl * 8, f32x2{-mu * r2, r2});
            }
        }
    }
    {
        lds_ptr cp = lds + K1_COLS + (wm * 4 + wn) * 256;
#pragma unroll
        for (int i = 0; i < NT; ++i) {
            if (TYPE == 1) { if (g == 0) lds_wrf(cp + i * 64 + l15 * 4, cs[i][0]); }
            else if (l15 == 0) lds_wrf4(cp + i * 64 + g * 16, cs[i]);
        }
    }
    LGKM_BAR();
    STAMP(lds, K1_LDS, 13);
    const int tok0 = mb * 128 + wm * 64;
    lds_ptr cp = lds + K1_COLS + ((1 - wm) * 4 + wn) * 256;
    if constexpr (TYPE == 1) {
        float c[4];
#pragma unroll
        for (int i = 0; i < 4; ++i) c[i] = cs[i][0] + lds_rdf(cp + i * 64 + l15 * 4);
#pragma unroll
        for (int mp = 0; mp < 2; ++mp) {
            f32x4 sa[2][2];
#pragma unroll
            for (int m = 0; m < 2; ++m) {
                sa[m][0] = lds_rdf4(lds + K1_ROWST + (wm * 64 + (2 * mp + m) * 16 + g * 4) * 8);
                sa[m][1] = lds_rdf4(lds + K1_ROWST + (wm * 64 + (2 * mp + m) * 16 + g * 4) * 8 + 16);
            }
            const int to = (tok0 >> 3) + mp * 4 + ((g & 1) * 2 + (g >> 1));
            char* vp = ws + WS_V + ((size_t)to * DM + (T0 - 80) * 16 + l15) * 16;
#pragma unroll
            for (int i = 0; i < 4; ++i) {
                unsigned pkd[2][2];
#pragma unroll
                for (int m = 0; m < 2; ++m) {
                    float v[4];
#pragma unroll
                    for (int r = 0; r < 4; ++r)
                        v[r] = __builtin_fmaf(acc[2 * mp + m][i][r], sa[m][r >> 1][(r & 1) * 2 + 1], sa[m][r >> 1][(r & 1) * 2] * c[i]);
                    pkd[m][0] = pk2(v[0], v[1]);
                    pkd[m][1] = pk2(v[2], v[3]);
                }
                auto s0 = __builtin_amdgcn_permlane16_swap(pkd[0][0], pkd[1][0], false, false);
                auto s1 = __builtin_amdgcn_permlane16_swap(pkd[0][1], pkd[1][1], false, false);
                if (live) *(u32x4*)(vp + i * 256) = u32x4{s0[0], s1[0], s0[1], s1[1]};
            }
        }
    } else {
        f32x2 sr[4];
        const float sc = (TYPE == 0 && T0 < 40) ? 0.22360679774997896f * 1.4426950408889634f : 1.0f;
#pragma unroll
        for (int m = 0; m < 4; ++m) {
            sr[m] = lds_rdf2(lds + K1_ROWST + (TYPE == 2 ? 1024 : 0) + (wm * 64 + (m ^ pm) * 16 + l15) * 8);
            if (TYPE == 0) sr[m] *= sc;
        }
        if constexpr (TYPE == 2) { PIN(bb[0]); PIN(bb[1]); PIN(bb[2]); }
        char* op = ws + (TYPE == 2 ? WS_H : WS_QK) + ((size_t)(2 * T0 + (g >> 1)) * SEQ + tok0 + (g & 1) * 16 + l15) * 16;
#pragma unroll
        for (int i = 0; i < NT; ++i) {
            const f32x4 c = cs[i] + lds_rdf4(cp + i * 64 + g * 16);
            const f32x2 c01 = {c[0], c[1]}, c23 = {c[2], c[3]};
            f32x2 b01 = {0.f, 0.f}, b23 = {0.f, 0.f};
            if constexpr (TYPE == 2) {
                b01 = f32x2{bf_lo(bb[i][0]), bf_hi(bb[i][0])};
                b23 = f32x2{bf_lo(bb[i][1]), bf_hi(bb[i][1])};
            }
#pragma unroll
            for (int mp = 0; mp < 2; ++mp) {
                unsigned pkd[2][2];
#pragma unroll
                for (int m = 0; m < 2; ++m) {
                    const f32x4 a = acc[2 * mp + m][i];
                    const f32x2 nm = {sr[2 * mp + m][0], sr[2 * mp + m][0]}, rr = {sr[2 * mp + m][1], sr[2 * mp + m][1]};
                    f32x2 u0, u1;
                    if constexpr (TYPE == 2) {
                        u0 = gelu_tanh2(__builtin_elementwise_fma(f32x2{a[0], a[1]}, rr, __builtin_elementwise_fma(nm, c01, b01)));
                        u1 = gelu_tanh2(__builtin_elementwise_fma(f32x2{a[2], a[3]}, rr, __builtin_elementwise_fma(nm, c23, b23)));
                    } else {
                        u0 = __builtin_elementwise_fma(f32x2{a[0], a[1]}, rr, nm * c01);
                        u1 = __builtin_elementwise_fma(f32x2{a[2], a[3]}, rr, nm * c23);
                    }
                    pkd[m][0] = pk2(u0[0], u0[1]);
                    pkd[m][1] = pk2(u1[0], u1[1]);
                }
                auto s0 = __builtin_amdgcn_permlane16_swap(pkd[0][0], pkd[1][0], false, false);
                auto s1 = __builtin_amdgcn_permlane16_swap(pkd[0][1], pkd[1][1], false, false);
                *(u32x4*)(op + (size_t)i * 2 * SEQ * 16 + ((2 * mp) ^ pm) * 256) = u32x4{s0[0], s1[0], s0[1], s1[1]};
            }
        }
    }
}

__global__ __launch_bounds__(512, 2) void k1_qkv_mlp1(const u16* __restrict__ x, const u16* __restrict__ wqkv, const u16* __restrict__ w1,
                                                      const u16* __restrict__ b1, const u16* __restrict__ wo, const u16* __restrict__ w2,
                                                      char* __restrict__ ws) {
    __shared__ __attribute__((aligned(1024))) char smem[K1_LDS + PROF_LDS];
    lds_ptr lds = (lds_ptr)smem;
    const unsigned lds0 = (unsigned)(uintptr_t)lds;
    const int lane = threadIdx.x & 63;
    const int w = __builtin_amdgcn_readfirstlane(threadIdx.x >> 6);
    const int b = blockIdx.x, xcd = b & 7, idx = b >> 3;
    const int mb = (xcd & 1) * 8 + (idx & 7), cg = (xcd >> 1) * 4 + (idx >> 3);
    const int cq = cg < 15 ? cg : 14;
    STAMP(lds, K1_LDS, 0);
    K1Dma dm;
    {
        dm.dst = lds0 + w * 1024;
        const int q8 = lane >> 3, c = lane & 7;
        const int r = 8 * w + q8;
        dm.vx = r * (DM * 2) + ((c ^ ((r >> 1) & 7)) * 16);
        dm.xg = (const char*)x + (size_t)mb * 128 * DM * 2;
        glds16(dm.vx, dm.xg, dm.dst);
        glds16(dm.vx, dm.xg + 64 * DM * 2, dm.dst + 8192);
        const int ko = w >> 1;
        const int a = (c >> 1) ^ (((q8 >> 1) & 1) | ((ko & 1) << 1));
        dm.vq = (8 * ko + q8) * (3 * DM * 2) + (w & 1) * 128 + a * 32 + (c & 1) * 16;
        dm.qg = (const char*)wqkv + cq * 256;
        glds16(dm.vq, dm.qg, dm.dst + 2 * 8192);
        glds16(dm.vq, dm.qg + 32 * 3 * DM * 2, dm.dst + 3 * 8192);
        unsigned vm[2];
#pragma unroll
        for (int i = 0; i < 2; ++i) {
            const int u = ((w + 8 * i) * 64 + lane) % 768;
            const int k2 = u / 96, rem = u - k2 * 96, row = rem / 12, c2 = rem - row * 12;
            vm[i] = (8 * k2 + row) * (FF * 2) + (((c2 >> 1) ^ (k2 & 1)) * 32) + (c2 & 1) * 16;
        }
        dm.vm0 = vm[0];
        dm.vm1 = vm[1];
        dm.mg = (const char*)w1 + cg * 192;
        glds16(dm.vm0, dm.mg, dm.dst + 4 * 8192);
        if (w < 4) glds16(dm.vm1, dm.mg, dm.dst + 5 * 8192);
        glds16(dm.vx, dm.xg + 128, dm.dst + K1_STAGE);
        glds16(dm.vx, dm.xg + 128 + 64 * DM * 2, dm.dst + K1_STAGE + 8192);
        glds16(dm.vq, dm.qg + 64 * 3 * DM * 2, dm.dst + K1_STAGE + 2 * 8192);
        glds16(dm.vq, dm.qg + 96 * 3 * DM * 2, dm.dst + K1_STAGE + 3 * 8192);
        glds16(dm.vm0, dm.mg + 64 * FF * 2, dm.dst + K1_STAGE + 4 * 8192);
        if (w < 4) glds16(dm.vm1, dm.mg + 64 * FF * 2, dm.dst + K1_STAGE + 5 * 8192);
    }
    const int pid0 = b + 256 * w, pid1 = pid0 + 2048;
    const bool t1 = pid1 < 2720;
    const int pl0 = pid0 / 10, cb0 = pid0 - pl0 * 10, pl1 = t1 ? pid1 / 10 : 0, cb1 = t1 ? pid1 - pl1 * 10 : 0;
    dm.tv = (lane >> 3) * (DM * 2) + (lane & 7) * 16;
    dm.tsrc0 = (pl0 < 80 ? (const char*)wo + (size_t)pl0 * 8 * DM * 2 : (const char*)w2 + (size_t)(pl0 - 80) * 8 * DM * 2) + cb0 * 128;
    dm.tsrc1 = (const char*)w2 + (size_t)(t1 ? pl1 - 80 : 0) * 8 * DM * 2 + cb1 * 128;
    dm.tdst0 = ws + WS_WT + ((size_t)pl0 * DM + cb0 * 64) * 16;
    dm.tdst1 = ws + WS_WT + ((size_t)pl1 * DM + cb1 * 64) * 16;
    dm.t1 = t1;
    dm.zdst = ws + WS_O + b * 10240;

    const int wm = w >> 2, wn = (w & 3) ^ (wm << 1);
    if (wn >= 2) k1_body<2>(lds, dm, wm, wn, 3 * (2 * cg + wn - 2), x, b1, ws, mb, lane, true);
    else if (cg >= 10) k1_body<1>(lds, dm, wm, wn, 4 * (2 * cq + wn), x, b1, ws, mb, lane, cg < 15);
    else k1_body<0>(lds, dm, wm, wn, 4 * (2 * cq + wn), x, b1, ws, mb, lane, true);
}

constexpr int K2_SLOT = 23552, K2_VOFF = 12288;
constexpr int K2_RING = 88 * 1024;
constexpr int K2_FLAG = K2_RING + 3 * K2_SLOT;
constexpr int K2_STG = 100 * 1024;
constexpr int K2_ONE = K2_FLAG - 1024 + 320;
constexpr int K2_LDS = 160 * 1024;
typedef __attribute__((address_space(1))) unsigned gu32;

#define VSTAMP(i) do {} while (0)

__device__ __forceinline__ void glds16m(unsigned voff, const void* sbase, unsigned lds_base, unsigned long long mask) {
    unsigned sv;
    asm volatile("s_mov_b32 %0, m0\n\ts_mov_b32 m0, %3\n\ts_mov_b64 exec, %4\n\tglobal_load_lds_dwordx4 %1, %2\n\ts_mov_b64 exec, -1\n\ts_mov_b32 m0, %0"
                 : "=&s"(sv) : "v"(voff), "s"(sbase), "s"(lds_base), "s"(mask) : "memory");
}

template <int I, int N, class F> __device__ __forceinline__ void sfor(F&& f) {
    if constexpr (I < N) { f(IC<I>{}); sfor<I + 1, N>(f); }
}

__global__ __launch_bounds__(512, 2) void k2_attn(const u16* __restrict__ x, const u16* __restrict__ b2, char* __restrict__ ws,
                                                  u16* __restrict__ out) {
    __shared__ __attribute__((aligned(1024))) char smem[K2_LDS];
    lds_ptr lds = (lds_ptr)smem;
    const unsigned lds0 = (unsigned)(uintptr_t)lds;
    const int tid = threadIdx.x, lane = tid & 63, r32 = lane & 31, hi = lane >> 5;
    const int w = __builtin_amdgcn_readfirstlane(tid >> 6);
    const int b = blockIdx.x, xcd = b & 7, idx = b >> 3;
    const int h = xcd * 4 + (idx >> 3), qb = idx & 7;
    const int odd = h & 1, plane0 = 5 * (h >> 1) + 2 * odd;
    const int mp = odd, xp = odd ? 0 : 2;
    const int tok = qb * 256 + w * 32 + r32;
    VSTAMP(0);
    u32x4 q0, q1;
    {
        const char* qp0 = ws + WS_QK + ((size_t)(plane0 + mp + hi) * SEQ + tok) * 16;
        const char* qp1 = ws + WS_QK + ((size_t)(plane0 + xp) * SEQ + tok) * 16;
        asm volatile("global_load_dwordx4 %0, %1, off" : "=v"(q0) : "v"(qp0) : "memory");
        asm volatile("global_load_dwordx4 %0, %1, off" : "=v"(q1) : "v"(qp1) : "memory");
    }
    unsigned doff[3], dstr[3];
    unsigned long long dmask[3];
#pragma unroll
    for (int i = 0; i < 3; ++i) {
        const int n = w + 8 * i;
        const int gl = lane < 63 ? lane / 21 : 0, d = lane < 63 ? lane - gl * 21 : 0;
        doff[i] = n < 12 ? (unsigned)WS_QK + ((80 + plane0 + (n >> 2)) * SEQ + (n & 3) * 64 + lane) * 16
                         : (unsigned)WS_V + ((3 * (n - 12) + gl) * DM + 20 * h + d) * 16;
        dstr[i] = n < 12 ? 256 * 16 : 32 * DM * 16;
        dmask[i] = n < 12 ? ~0ull : n < 22 ? ~((1ull << 20) | (1ull << 41) | (3ull << 62)) : ((1ull << 41) - 1) & ~(1ull << 20);
    }
    const int kap = (r32 & 19) | ((r32 & 4) << 1) | ((r32 & 8) >> 1);
    const int mb = qb * 4 + (xcd & 3), nb = (xcd >> 2) * 4 + (idx >> 3), th = w & 1;
    const __amdgpu_buffer_rsrc_t rs = __builtin_amdgcn_make_buffer_rsrc(ws, 0, (unsigned)WS_END, 0x00020000);
    const unsigned hoff = ((lane >> 4) * SEQ + (lane & 15)) * 16, woff = ((lane >> 4) * DM + (lane & 15)) * 16;
    u32x4 fr[54];
    unsigned hbase, wbase;
    auto ldfrag = [&](auto F_) {
        constexpr int F = decltype(F_)::v, ks = F / 9, i = F % 9;
        if constexpr (i < 5) fr[F] = __builtin_amdgcn_raw_buffer_load_b128(rs, woff, wbase + ks * 4 * DM * 16 + i * 256, 0);
        else fr[F] = __builtin_amdgcn_raw_buffer_load_b128(rs, hoff, (hbase ^ (((i - 5) >> 1) * 512)) + ks * 4 * SEQ * 16 + ((i - 5) & 1) * 256, 0);
    };
    unsigned km = K2_RING + K2_SLOT + (mp + hi) * 4096 + kap * 16, kx = hi ? K2_ONE : K2_RING + K2_SLOT + xp * 4096 + kap * 16, vb[3];
#pragma unroll
    for (int r = 0; r < 3; ++r) vb[r] = K2_RING + K2_SLOT + K2_VOFF + (r32 > 20 ? 1008 - 336 * r : r32 * 16 + hi * (r == 2 ? 352 : 336));
    f32x4 acc[4][5];
    auto kstep = [&](auto KS_, auto&& act) {
        constexpr int ks = decltype(KS_)::v;
        sfor<0, 20>([&](auto J_) {
            constexpr int j = decltype(J_)::v, m = j / 5, i = j % 5;
            acc[m][i] = mfma16(fr[9 * ks + i], fr[9 * ks + 5 + m], ks ? acc[m][i] : f32x4{0.f, 0.f, 0.f, 0.f});
            act(J_);
            SBAR();
        });
    };
    u32x4 wq[5][5];
    constexpr int NQ3 = 7, NL0 = 1;
    const f32x16 zero = {0.f, 0.f, 0.f, 0.f, 0.f, 0.f, 0.f, 0.f, 0.f, 0.f, 0.f, 0.f, 0.f, 0.f, 0.f, 0.f};
    {
        unsigned lw = lds0 + w * 1024;
        hbase = (unsigned)WS_H + (24 * w * SEQ + mb * 64 + th * 32) * 16;
        wbase = (unsigned)WS_WT + ((80 + 24 * w) * DM + nb * 80) * 16;
        asm volatile("" : "+s"(lw), "+s"(hbase), "+s"(wbase));
        auto piece = [&](int i, unsigned slot) {
            if (i < 2 || w < 7) glds16m(doff[i], ws, lw + K2_RING + slot + 8 * i * 1024, dmask[i]);
            doff[i] += dstr[i];
            PIN(doff[i]);
        };
        auto wopiece = [&](int r) {
            const unsigned p = 8 * r + w, ks = (p * 205u) >> 10, i = p - 5 * ks;
            glds16(woff, ws + WS_WT + (ks * 4 * DM + nb * 80 + i * 16) * 16, lw + r * 8192);
        };
#pragma unroll
        for (int c = 0; c < 2; ++c)
#pragma unroll
            for (int i = 0; i < 3; ++i) piece(i, (c + 1) * K2_SLOT);
        if (tid < 132) {
            const int sl = tid / 44, r = tid - sl * 44;
            unsigned one = (r & 3) == 3 ? 0u : ONES2;
            PIN(one);
            *(__attribute__((address_space(3))) u32x4*)(lds + K2_RING + sl * K2_SLOT + K2_VOFF + (r >> 2) * 1024 + ((r & 3) == 3 ? 1008 : 320 + (r & 3) * 336)) = u32x4{one, one, one, one};
        }
        if (tid >= 192 && tid < 199) *(__attribute__((address_space(3))) u32x4*)(lds + K2_ONE + (tid - 191) * 512) = u32x4{ONES2, ONES2, ONES2, ONES2};
        asm volatile("s_waitcnt vmcnt(2) lgkmcnt(0)\n\ts_barrier" : "+v"(q0), "+v"(q1) :: "memory");
        VSTAMP(1);
        __builtin_amdgcn_s_setprio(3);
        if (hi) q1 = u32x4{0u, 0u, 0u, 0u};
        else if (odd) { q1[0] = 0u; q1[1] = 0u; }
        else { q1[2] = 0u; q1[3] = 0u; }
        u32x4 kf0 = lds_rd128(lds + km), kf1 = lds_rd128(lds + kx), vf0, vf1, p0, p1;
        f32x16 s[2], o;
        s[0] = mfma32(kf0, q0, zero);
        s[0] = mfma32(kf1, q1, s[0]);
        kf0 = lds_rd128(lds + km + 512);
        kf1 = lds_rd128(lds + kx + 512);
        vf0 = lds_rd128(lds + vb[0]);
        {
            unsigned z = 0u;
            PIN(z);
            vf1 = p1 = u32x4{z, z, z, z};
#pragma unroll
            for (int r = 0; r < 16; ++r) o[r] = __uint_as_float(z);
        }
        {
            float m = s[0][0];
#pragma unroll
            for (int r = 1; r < 16; ++r) m = __builtin_fmaxf(m, s[0][r]);
            auto rr = __builtin_amdgcn_permlane32_swap(__float_as_uint(m), __float_as_uint(m), false, false);
            m = __builtin_fmaxf(__uint_as_float(rr[0]), __uint_as_float(rr[1]));
#pragma unroll
            for (int r = 0; r < 16; ++r) s[0][r] -= m;
            if (hi) {
                const unsigned a = pk2(-m, 0.f);
                const float r1 = -m - bf_lo(a);
                const unsigned c = pk2(r1, 0.f);
                q1[0] = a | (c << 16);
                q1[1] = pk2(r1 - bf_lo(c), 0.f);
            }
            PIN(q1);
        }
        SBAR();
        auto tile = [&](auto TT_, auto KIND_, int c, unsigned step, unsigned nslot) {
            constexpr int TT = decltype(TT_)::v, KIND = decltype(KIND_)::v, CU = TT & 1, NX = CU ^ 1;
            auto extra = [&](auto G_) {
                constexpr int G = decltype(G_)::v;
                if constexpr (KIND == 0) {
                    if constexpr (G == 1 && TT == 1) wopiece(2 * c);
                    if constexpr (G == 1 && TT == 3) { if (c < 5) wopiece(2 * c + 1); }
                    if constexpr (TT >= 6 && (G & 1) && (TT - 6) * 2 + (G >> 1) < 3) piece((TT - 6) * 2 + (G >> 1), nslot);
                } else {
                    constexpr int n = TT < 4 ? 3 * TT + G : 4 + 2 * TT + (G >> 1);
                    if constexpr (!(G & 1) || (G == 1 && TT < 4)) ldfrag(IC<(KIND - 1) * 20 + n>{});
                    constexpr int r = G != 3 ? -1 : KIND == 1 ? (TT == 6 ? 0 : -1) : (TT >= 1 && TT <= 3 ? TT : -1);
                    if constexpr (r >= 0) glds16(hoff, ws + ((hbase ^ ((r >> 1) * 512)) + 5 * 4 * SEQ * 16 + (r & 1) * 256), lw + K2_STG + r * 8192);
                    if constexpr (KIND == 1 && G == 1 && TT == 6) wopiece(11);
                    if constexpr (KIND == 1 && G == 1 && TT == 7) { if (w < 4) wopiece(12); }
                }
                SBAR();
            };
            auto soft = [&](auto G_) {
                constexpr int G = decltype(G_)::v;
                u32x4& p = G < 2 ? p0 : p1;
                float e0 = __builtin_amdgcn_exp2f(s[CU][4 * G]), e1 = __builtin_amdgcn_exp2f(s[CU][4 * G + 1]);
                PIN(e0); PIN(e1);
                SBAR();
                extra(G_);
                float e2 = __builtin_amdgcn_exp2f(s[CU][4 * G + 2]), e3 = __builtin_amdgcn_exp2f(s[CU][4 * G + 3]);
                PIN(e2); PIN(e3);
                SBAR();
                p[2 * (G & 1)] = pk2(e0, e1);
                p[2 * (G & 1) + 1] = pk2(e2, e3);
                PIN(p);
                SBAR();
            };
            if constexpr (KIND == 2 && TT == 1) asm volatile("s_barrier" ::: "memory");
            if constexpr (KIND == 2 && TT == 5) asm volatile("s_waitcnt vmcnt(18)\n\ts_barrier" ::: "memory");
            if constexpr (TT == 6) {
                if constexpr (KIND == 0) asm volatile("s_waitcnt vmcnt(1)\n\ts_barrier" ::: "memory");
                else if constexpr (KIND == 1) asm volatile("s_waitcnt vmcnt(16)\n\ts_barrier" ::: "memory");
                km += step;
                PIN(km);
                asm volatile("s_mov_b32 exec_hi, 0\n\tv_add_u32 %0, %1, %0\n\ts_mov_b32 exec_hi, -1" : "+v"(kx) : "s"(step));
                SBAR();
            }
            s[NX] = mfma32(kf0, q0, zero);
            kf0 = lds_rd128(lds + km + ((TT + 2) & 7) * 512);
            SBAR();
            soft(IC<0>{});
            s[NX] = mfma32(kf1, q1, s[NX]);
            kf1 = lds_rd128(lds + kx + ((TT + 2) & 7) * 512);
            SBAR();
            soft(IC<1>{});
            o = mfma32(vf1, p1, o);
            vf1 = lds_rd128(lds + vb[(4 * TT + 2) % 3] + ((4 * TT + 2) / 3) * 1024 + ((4 * TT + 2) % 3) * 336);
            SBAR();
            soft(IC<2>{});
            o = mfma32(vf0, p0, o);
            if constexpr (TT == 7) {
#pragma unroll
                for (int r = 0; r < 3; ++r) { vb[r] += step; PIN(vb[r]); }
            }
            constexpr int GN = (4 * TT + 4) & 31;
            vf0 = lds_rd128(lds + vb[GN % 3] + (GN / 3) * 1024 + (GN % 3) * 336);
            SBAR();
            soft(IC<3>{});
        };
        auto body = [&](auto KIND_, int c, unsigned step, unsigned nslot) {
            tile(IC<0>{}, KIND_, c, step, nslot); tile(IC<1>{}, KIND_, c, step, nslot); tile(IC<2>{}, KIND_, c, step, nslot);
            tile(IC<3>{}, KIND_, c, step, nslot); tile(IC<4>{}, KIND_, c, step, nslot); tile(IC<5>{}, KIND_, c, step, nslot);
            tile(IC<6>{}, KIND_, c, step, nslot); tile(IC<7>{}, KIND_, c, step, nslot);
        };
        unsigned nslot = 0;
#pragma unroll 1
        for (int c = 0; c < 6; ++c) {
            body(IC<0>{}, c, nslot == (unsigned)K2_SLOT ? (unsigned)(-2 * K2_SLOT) : (unsigned)K2_SLOT, nslot);
            nslot = nslot == 2u * K2_SLOT ? 0u : nslot + K2_SLOT;
            VSTAMP(2 + c);
        }
        body(IC<1>{}, 6, K2_SLOT, 0);
        VSTAMP(8);
        body(IC<2>{}, 7, -K2_SLOT, 0);
        VSTAMP(9);
        o = mfma32(vf1, p1, o);
        int ln = lane;
        PIN(ln);
        auto rs0 = __builtin_amdgcn_permlane32_swap(__float_as_uint(o[8]), __float_as_uint(o[8]), false, false);
        float l = __uint_as_float(rs0[1]);
        bool ok = l <= 1.2676506e30f;
#pragma unroll
        for (int r = 0; r < 12; ++r) ok = ok && (__builtin_fabsf(o[r]) < __builtin_inff());
        if (__builtin_amdgcn_ballot_w64(!ok)) {
            const int r5 = ln & 31, hh = ln >> 5;
            const unsigned kk = ((r5 & 19) | ((r5 & 4) << 1) | ((r5 & 8) >> 1)) * 16;
            const unsigned vo = (hh * DM + 20 * h + (r5 < 20 ? r5 : 0)) * 16, fill = r5 == 20 ? ONES2 : 0u;
            if (hh) q1 = u32x4{0u, 0u, 0u, 0u};
            auto score = [&](int t, const f32x16& c) {
                const u32x4 ka = __builtin_amdgcn_raw_buffer_load_b128(rs, hh * SEQ * 16 + kk, (unsigned)WS_QK + (80 + plane0 + mp) * SEQ * 16 + t * 512, 0);
                const u32x4 kb = __builtin_amdgcn_raw_buffer_load_b128(rs, kk, (unsigned)WS_QK + (80 + plane0 + xp) * SEQ * 16 + t * 512, 0);
                return mfma32(kb, q1, mfma32(ka, q0, c));
            };
            float mx = -__builtin_inff();
#pragma unroll 1
            for (int t = 0; t < 64; ++t) {
                const f32x16 z = score(t, zero);
#pragma unroll
                for (int r = 0; r < 16; ++r) mx = __builtin_fmaxf(mx, z[r]);
            }
            auto rr = __builtin_amdgcn_permlane32_swap(__float_as_uint(mx), __float_as_uint(mx), false, false);
            mx = __builtin_fmaxf(__uint_as_float(rr[0]), __uint_as_float(rr[1]));
            o = zero;
#pragma unroll 1
            for (int t = 0; t < 64; ++t) {
                const f32x16 z = score(t, zero);
#pragma unroll
                for (int j = 0; j < 2; ++j) {
                    u32x4 v = __builtin_amdgcn_raw_buffer_load_b128(rs, vo, (unsigned)WS_V + (4 * t + 2 * j) * DM * 16, 0), p;
#pragma unroll
                    for (int r = 0; r < 4; ++r) {
                        if (r5 >= 20) v[r] = fill;
                        p[r] = pk2(__builtin_amdgcn_exp2f(z[8 * j + 2 * r] - mx), __builtin_amdgcn_exp2f(z[8 * j + 2 * r + 1] - mx));
                    }
                    o = mfma32(v, p, o);
                }
            }
            auto r2 = __builtin_amdgcn_permlane32_swap(__float_as_uint(o[8]), __float_as_uint(o[8]), false, false);
            l = __uint_as_float(r2[1]);
        }
        {
            const float inv = 1.0f / l;
            const unsigned ooff = (ln & 31) * 16 + (odd ? (ln >> 5 ? SEQ * 16 : 8) : (ln >> 5) * 8);
#pragma unroll
            for (int qd = 0; qd < 3; ++qd) {
                u32x2 v = {pk2(o[4 * qd] * inv, o[4 * qd + 1] * inv), pk2(o[4 * qd + 2] * inv, o[4 * qd + 3] * inv)};
                if (v[0] == 0u) v[0] = 0x80000000u;
                if (qd < 2 || ln < 32) __builtin_amdgcn_raw_buffer_store_b64(v, rs, ooff, (unsigned)WS_O + ((plane0 + qd) * SEQ + qb * 256 + w * 32) * 16, 16);
            }
        }
    }
    int ln = lane;
    PIN(ln);
    SBAR();
    VSTAMP(18);
    __builtin_amdgcn_s_setprio(0);
    kstep(IC<0>{}, [&](auto J_) { constexpr int j = decltype(J_)::v; if constexpr (j >= 20 - NL0) ldfrag(IC<40 + j - (20 - NL0)>{}); });
    VSTAMP(19);
    kstep(IC<1>{}, [&](auto J_) { constexpr int j = decltype(J_)::v; if constexpr (j % 2 == 0 && 40 + NL0 + j / 2 < 50) ldfrag(IC<40 + NL0 + j / 2>{}); });
    VSTAMP(21);
    asm volatile("s_waitcnt vmcnt(18)" ::: "memory");
    kstep(IC<2>{}, [&](auto J_) { constexpr int j = decltype(J_)::v; if constexpr (j % 2 == 0 && j < 8) fr[50 + j / 2] = lds_rd128(lds + K2_STG + w * 1024 + ln * 16 + (j / 2) * 8192); });
    VSTAMP(22);
    const unsigned wqb = (w >> 1) * 25600 + ln * 16;
    u32x4 of[10];
    const unsigned obase = (unsigned)WS_O + (20 * (w >> 1) * SEQ + mb * 64 + th * 32) * 16;
    auto ldo = [&](auto J_) {
        constexpr int j = decltype(J_)::v;
        of[j] = __builtin_amdgcn_raw_buffer_load_b128(rs, hoff, obase + (j % 5) * 4 * SEQ * 16 + (j / 5) * 256, 16);
    };
    kstep(IC<3>{}, [&](auto J_) { constexpr int j = decltype(J_)::v; if constexpr (j % 2 == 0 && j < 2 * NQ3) wq[j / 10][(j / 2) % 5] = lds_rd128(lds + wqb + (j / 2) * 1024); });
    VSTAMP(11);
    kstep(IC<4>{}, [&](auto J_) { constexpr int j = decltype(J_)::v; if constexpr (j % 2 == 0) ldo(IC<j / 2>{}); });
    VSTAMP(23);
    kstep(IC<5>{}, [&](auto J_) {
        constexpr int j = decltype(J_)::v, n = NQ3 + j / 2;
        if constexpr (j % 2 == 0 && j < 18) wq[n / 5][n % 5] = lds_rd128(lds + wqb + n * 1024);
    });
    sfor<NQ3 + 9, 25>([&](auto J_) { constexpr int j = decltype(J_)::v; wq[j / 5][j % 5] = lds_rd128(lds + wqb + j * 1024); });
    asm volatile("s_waitcnt lgkmcnt(0)\n\ts_barrier" ::: "memory");
    VSTAMP(12);
    const unsigned rw = w * 20480 + ln * 16, rwa = rw + (th ^ 1) * 10240, rwb = rw + th * 10240;
#pragma unroll
    for (int j = 0; j < 10; ++j) lds_wrf4(lds + rwa + j * 1024, acc[2 + j / 5][j % 5]);
    SBAR();
    for (unsigned spins = 0; spins < (1u << 17); ++spins) {
        unsigned pend = 0u;
        sfor<0, 10>([&](auto J_) {
            constexpr int j = decltype(J_)::v;
            if (__builtin_amdgcn_ballot_w64(__builtin_elementwise_min(of[j][0], of[j][2]) == 0u)) pend |= 1u << j;
        });
        if (!pend) break;
        asm volatile("" ::: "memory");
        sfor<0, 10>([&](auto J_) { if (pend >> decltype(J_)::v & 1u) ldo(J_); });
    }
    VSTAMP(13);
    unsigned eoff[3];
    u32x2 xv[3], bv[3];
#pragma unroll
    for (int r = 0; r < 3; ++r) {
        const unsigned u = 160 * w + 64 * r + (r < 2 ? ln : ln & 31), sub = u >> 6, m = (sub * 205u) >> 10, i = sub - 5 * m;
        const unsigned col = nb * 80 + i * 16 + ((u >> 4) & 3) * 4;
        eoff[r] = ((mb * 64 + m * 16 + (u & 15)) * DM + col) * 2;
        xv[r] = *(const u32x2*)((const char*)x + eoff[r]);
        bv[r] = *(const u32x2*)((const char*)b2 + col * 2);
    }
    SBAR();
    sfor<0, 25>([&](auto J_) {
        constexpr int j = decltype(J_)::v;
        acc[0][j % 5] = mfma16(wq[j / 5][j % 5], of[j / 5], acc[0][j % 5]);
        SBAR();
    });
    sfor<0, 25>([&](auto J_) {
        constexpr int j = decltype(J_)::v, i = j < 10 ? j % 2 : 2 + (j - 10) % 3, k = j < 10 ? j / 2 : (j - 10) / 3;
        acc[1][i] = mfma16(wq[k][i], of[5 + k], acc[1][i]);
        if constexpr (j % 2 == 1 && j < 10) lds_wrf4(lds + rwb + (j / 2) * 1024, acc[0][j / 2]);
        if constexpr (j == 13 || j == 16) lds_wrf4(lds + rwb + (5 + (j - 13) / 3) * 1024, acc[1][(j - 13) / 3]);
        SBAR();
    });
    VSTAMP(14);
#pragma unroll
    for (int i = 2; i < 5; ++i) lds_wrf4(lds + rwb + (5 + i) * 1024, acc[1][i]);
    asm volatile("s_waitcnt lgkmcnt(0)\n\ts_barrier" ::: "memory");
    VSTAMP(15);
#pragma unroll
    for (int r = 0; r < 3; ++r) {
        lds_ptr p = lds + (160 * w + 64 * r + (r < 2 ? ln : ln & 31)) * 16;
        f32x4 t[8];
#pragma unroll
        for (int k = 0; k < 8; ++k) t[k] = lds_rdf4(p + k * 20480);
        const f32x4 s = ((t[0] + t[1]) + (t[2] + t[3])) + ((t[4] + t[5]) + (t[6] + t[7]));
        u32x2 v;
        v[0] = pk2(s[0] + bf_lo(xv[r][0]) + bf_lo(bv[r][0]), s[1] + bf_hi(xv[r][0]) + bf_hi(bv[r][0]));
        v[1] = pk2(s[2] + bf_lo(xv[r][1]) + bf_lo(bv[r][1]), s[3] + bf_hi(xv[r][1]) + bf_hi(bv[r][1]));
        if (r < 2 || ln < 32) *(u32x2*)((char*)out + eoff[r]) = v;
    }
}

extern "C" void kernel_launch(void* const* d_in, const int* in_sizes, int n_in, void* d_out, int out_size, void* d_ws, size_t ws_size,
                              hipStream_t stream) {
    const u16* x = (const u16*)d_in[0];
    const u16* wqkv = (const u16*)d_in[1];
    const u16* wo = (const u16*)d_in[2];
    const u16* w1 = (const u16*)d_in[4];
    const u16* b1 = (const u16*)d_in[5];
    const u16* w2 = (const u16*)d_in[6];
    const u16* b2 = (const u16*)d_in[7];
    char* ws = (char*)d_ws;
    k1_qkv_mlp1<<<256, 512, 0, stream>>>(x, wqkv, w1, b1, wo, w2, ws);
    k2_attn<<<256, 512, 0, stream>>>(x, b2, ws, (u16*)d_out);
}
```

```cpp
#include <hip/hip_runtime.h>
#include <stdint.h>

typedef __attribute__((ext_vector_type(8))) __bf16 bf16x8;
typedef __attribute__((ext_vector_type(2))) __bf16 bf16x2;
typedef __attribute__((ext_vector_type(4))) float f32x4;
typedef __attribute__((ext_vector_type(2))) float f32x2;
typedef __attribute__((ext_vector_type(16))) float f32x16;
typedef __attribute__((ext_vector_type(4))) unsigned u32x4;
typedef __attribute__((ext_vector_type(2))) unsigned u32x2;
typedef short v4i16_t __attribute__((ext_vector_type(4)));
typedef __attribute__((address_space(3))) char* lds_ptr;
typedef unsigned short u16;
template <int V> struct IC { static constexpr int v = V; };

#define SBAR() __builtin_amdgcn_sched_barrier(0)
#define PIN(x) asm volatile("" : "+v"(x))
#define WAIT_BAR(N) asm volatile("s_waitcnt vmcnt(" #N ") lgkmcnt(0)\n\ts_barrier" ::: "memory")
#define WAIT_VM(N) asm volatile("s_waitcnt vmcnt(" #N ")" ::: "memory")
#define LGKM_BAR() asm volatile("s_waitcnt lgkmcnt(0)\n\ts_barrier" ::: "memory")

constexpr int SEQ = 2048, DM = 640, FF = 1536;
constexpr size_t WS_QK = 0;
constexpr size_t WS_V = WS_QK + (size_t)160 * SEQ * 16;
constexpr size_t WS_O = WS_V + (size_t)256 * DM * 16;
constexpr size_t WS_H = WS_O + (size_t)80 * SEQ * 16;
constexpr size_t WS_WT = WS_H + (size_t)192 * SEQ * 16;
constexpr size_t WS_CNT = WS_WT + (size_t)272 * DM * 16;
constexpr size_t WS_END = WS_CNT + (size_t)160 * SEQ * 16;

__device__ __forceinline__ void glds16(unsigned voff, const void* sbase, unsigned lds_base) {
    unsigned sv;
    asm volatile("s_mov_b32 %0, m0\n\ts_mov_b32 m0, %3\n\ts_nop 0\n\tglobal_load_lds_dwordx4 %1, %2\n\ts_mov_b32 m0, %0"
                 : "=&s"(sv) : "v"(voff), "s"(sbase), "s"(lds_base) : "memory");
}
__device__ __forceinline__ void glds16m(unsigned voff, const void* sbase, unsigned lds_base, unsigned long long mask) {
    unsigned sv;
    asm volatile("s_mov_b32 %0, m0\n\ts_mov_b32 m0, %3\n\ts_mov_b64 exec, %4\n\tglobal_load_lds_dwordx4 %1, %2\n\ts_mov_b64 exec, -1\n\ts_mov_b32 m0, %0"
                 : "=&s"(sv) : "v"(voff), "s"(sbase), "s"(lds_base), "s"(mask) : "memory");
}
__device__ __forceinline__ u32x2 tr_read(lds_ptr p) {
    return __builtin_bit_cast(u32x2, __builtin_amdgcn_ds_read_tr16_b64_v4i16((__attribute__((address_space(3))) v4i16_t*)p));
}
__device__ __forceinline__ u32x4 lds_rd128(lds_ptr p) { return *(__attribute__((address_space(3))) u32x4*)p; }
__device__ __forceinline__ f32x4 lds_rdf4(lds_ptr p) { return *(__attribute__((address_space(3))) f32x4*)p; }
__device__ __forceinline__ f32x2 lds_rdf2(lds_ptr p) { return *(__attribute__((address_space(3))) f32x2*)p; }
__device__ __forceinline__ float lds_rdf(lds_ptr p) { return *(__attribute__((address_space(3))) float*)p; }
__device__ __forceinline__ void lds_wrf4(lds_ptr p, f32x4 v) { *(__attribute__((address_space(3))) f32x4*)p = v; }
__device__ __forceinline__ void lds_wrf2(lds_ptr p, f32x2 v) { *(__attribute__((address_space(3))) f32x2*)p = v; }
__device__ __forceinline__ void lds_wrf(lds_ptr p, float v) { *(__attribute__((address_space(3))) float*)p = v; }
__device__ __forceinline__ f32x4 mfma16(u32x4 a, u32x4 b, f32x4 c) {
    return __builtin_amdgcn_mfma_f32_16x16x32_bf16(__builtin_bit_cast(bf16x8, a), __builtin_bit_cast(bf16x8, b), c, 0, 0, 0);
}
__device__ __forceinline__ f32x16 mfma32(u32x4 a, u32x4 b, f32x16 c) {
    return __builtin_amdgcn_mfma_f32_32x32x16_bf16(__builtin_bit_cast(bf16x8, a), __builtin_bit_cast(bf16x8, b), c, 0, 0, 0);
}
__device__ __forceinline__ unsigned pk2(float lo, float hi) {
    bf16x2 v = {(__bf16)lo, (__bf16)hi};
    return __builtin_bit_cast(unsigned, v);
}
__device__ __forceinline__ float bf_lo(unsigned u) { return __uint_as_float(u << 16); }
__device__ __forceinline__ float bf_hi(unsigned u) { return __uint_as_float(u & 0xffff0000u); }
constexpr unsigned ONES2 = 0x3f803f80u;

constexpr int K1_STAGE = 45056, K1_QOFF = 16384, K1_MOFF = 32768;
constexpr int K1_ROWST = 3 * K1_STAGE;
constexpr int K1_COLS = K1_ROWST + 2048;
constexpr int K1_TR = K1_COLS + 2048;
constexpr int K1_LDS = K1_TR + 16 * 1024;

__device__ __forceinline__ f32x2 gelu_tanh2(f32x2 u) {
    const float c1 = -2.0f * 0.7978845608028654f * 1.4426950408889634f;
    const float c2 = c1 * 0.044715f;
    f32x2 t = u * __builtin_elementwise_fma(u * u, f32x2{c2, c2}, f32x2{c1, c1});
    f32x2 d = f32x2{__builtin_amdgcn_exp2f(t[0]), __builtin_amdgcn_exp2f(t[1])} + f32x2{1.0f, 1.0f};
    return u * f32x2{__builtin_amdgcn_rcpf(d[0]), __builtin_amdgcn_rcpf(d[1])};
}

__device__ __forceinline__ void st16(char* ws, size_t off, int imm, u32x4 v) {
    const __amdgpu_buffer_rsrc_t rs = __builtin_amdgcn_make_buffer_rsrc(ws, 0, (unsigned)WS_END, 0x00020000);
    __builtin_amdgcn_raw_buffer_store_b128(v, rs, (unsigned)off + imm, 0, 16);
}

struct K1Dma { unsigned vx, vq, vm0, vm1, tv; const char *xg, *qg, *mg, *tsrc0, *tsrc1; char *tdst0, *tdst1; unsigned dst; bool t1; unsigned long long lo; };

template <int TYPE>
__device__ __forceinline__ void k1_loop(lds_ptr lds, const K1Dma& dm, const unsigned (&xa)[2][2], const unsigned (&wa)[2][4],
                                        f32x4 (&acc)[4][4], f32x4 (&cs)[4], f32x4 (&st)[4], const u16* bp, u32x2 (&bb)[3]) {
    constexpr int NT = TYPE == 2 ? 3 : 4;
    constexpr int HI = TYPE == 2 ? 768 : 512;
    constexpr int NR = 4 + 2 * NT;
    const u32x4 ones = {ONES2, ONES2, ONES2, ONES2};
    u32x4 xf[2][4];
    u32x2 wl[2][4], wh[2][4];
    auto piece = [&](int j, int s, unsigned so) {
        const unsigned d = dm.dst + so + j * 8192;
        if (j == 0) glds16(dm.vx, dm.xg + s * 128, d);
        if (j == 1) glds16(dm.vx, dm.xg + s * 128 + 64 * DM * 2, d);
        if (j == 2) glds16(dm.vq, dm.qg + (size_t)s * (64 * 3 * DM * 2), d);
        if (j == 3) glds16(dm.vq, dm.qg + (size_t)s * (64 * 3 * DM * 2) + 32 * 3 * DM * 2, d);
        if (j == 4) glds16(dm.vm0, dm.mg + (size_t)s * (64 * FF * 2), d);
        if (j == 5) glds16m(dm.vm1, dm.mg + (size_t)s * (64 * FF * 2), d, dm.lo);
    };
    auto rd = [&](auto F_, int n, unsigned so) {
        constexpr int F = decltype(F_)::v;
        const int k = n == 0 ? 0 : n < 3 ? n + 3 : n < 6 ? n - 2 : n;
        if (k < 4) xf[F][k] = lds_rd128(lds + so + xa[F][k >> 1] + (k & 1) * 2048);
        else if (((k - 4) & 1) == 0) wl[F][(k - 4) >> 1] = tr_read(lds + so + wa[F][(k - 4) >> 1]);
        else wh[F][(k - 4) >> 1] = tr_read(lds + so + wa[F][(k - 4) >> 1] + HI);
    };
    auto mm = [&](auto F_, int n) {
        constexpr int F = decltype(F_)::v;
        if (n < 4 * NT) {
            const int i = n >> 2, m = n & 3;
            const u32x4 wf = {wl[F][i][0], wl[F][i][1], wh[F][i][0], wh[F][i][1]};
            acc[m][i] = TYPE == 1 ? mfma16(xf[F][m], wf, acc[m][i]) : mfma16(wf, xf[F][m], acc[m][i]);
        } else if (TYPE == 2 && n < 16) {
            const int m = n & 1;
            if (n < 14) st[m] = mfma16(ones, xf[F][m], st[m]);
            else st[2 + m] = mfma16(xf[F][m], xf[F][m], st[2 + m]);
        } else {
            const int i = n - 16;
            const u32x4 wf = {wl[F][i][0], wl[F][i][1], wh[F][i][0], wh[F][i][1]};
            cs[i] = TYPE == 1 ? mfma16(ones, wf, cs[i]) : mfma16(wf, ones, cs[i]);
        }
    };
    auto phase = [&](auto F_, auto RD_, auto DMA_, unsigned rso, int ds, unsigned dso) {
        constexpr int F = decltype(F_)::v;
        constexpr int NM = F == 0 ? 16 + NT : 16;
#pragma unroll
        for (int n = 0; n < NM; ++n) {
            mm(F_, n);
            if (decltype(RD_)::v && n < NR) rd(IC<1 - F>{}, n, rso);
            SBAR();
            if (decltype(DMA_)::v && n % 3 == 0 && n / 3 < 6) { piece(n / 3, ds, dso); SBAR(); }
        }
    };
    WAIT_BAR(5);
#pragma unroll
    for (int n = 0; n < NR; ++n) rd(IC<0>{}, n, 0);
    phase(IC<0>{}, IC<1>{}, IC<1>{}, 0, 2, 2 * K1_STAGE);
    WAIT_BAR(5);
    unsigned so = 0;
#pragma unroll 1
    for (int s = 0; s < 7; ++s) {
        const unsigned sn = so == 2 * K1_STAGE ? 0 : so + K1_STAGE;
        phase(IC<1>{}, IC<1>{}, IC<1>{}, sn, s + 3, so);
        phase(IC<0>{}, IC<1>{}, IC<0>{}, sn, 0, 0);
        WAIT_BAR(5);
        so = sn;
    }
    for (int s = 7; s < 9; ++s) {
        const unsigned sn = so == 2 * K1_STAGE ? 0 : so + K1_STAGE;
        phase(IC<1>{}, IC<1>{}, IC<0>{}, sn, 0, 0);
        phase(IC<0>{}, IC<1>{}, IC<0>{}, sn, 0, 0);
        if (s == 7) {
            WAIT_BAR(0);
            glds16(dm.tv, dm.tsrc0, dm.dst + K1_TR);
            glds16(dm.tv, dm.tsrc1, dm.dst + K1_TR + 8192);
            if constexpr (TYPE == 2) {
                asm volatile("global_load_dwordx2 %0, %3, off\n\tglobal_load_dwordx2 %1, %3, off offset:32\n\tglobal_load_dwordx2 %2, %3, off offset:64"
                             : "=&v"(bb[0]), "=&v"(bb[1]), "=&v"(bb[2]) : "v"(bp) : "memory");
            }
        }
        so = sn;
    }
    phase(IC<1>{}, IC<0>{}, IC<0>{}, 0, 0, 0);
    if constexpr (TYPE == 2) asm volatile("s_waitcnt vmcnt(0)" : "+v"(bb[0]), "+v"(bb[1]), "+v"(bb[2]) :: "memory");
}

template <int TYPE>
__device__ __forceinline__ void k1_body(lds_ptr lds, const K1Dma& dm, int wm, int wn, int T0, const u16* x, const u16* b1, char* ws,
                                        int mb, int lane, bool live) {
    constexpr int NT = TYPE == 2 ? 3 : 4;
    const int g = lane >> 4, l15 = lane & 15;
    const int pm = TYPE == 2 ? 2 * (wn & 1) : 0;
    u32x2 bb[3];
    const u16* bp = b1 + T0 * 16 + g * 4;
    unsigned xa[2][2], wa[2][4];
    {
        const int t = wm * 64 + l15, f = (t >> 1) & 7;
        const int q = (lane >> 2) & 3, p = lane & 3;
#pragma unroll
        for (int F = 0; F < 2; ++F) {
            const int ks = wm ^ F;
            const unsigned xb = t * 128 + (((4 * ks + g) ^ f) * 16);
            xa[F][0] = xb + pm * 2048;
            xa[F][1] = xb + (2 - pm) * 2048;
#pragma unroll
            for (int a = 0; a < 4; ++a) {
                if (TYPE == 2) wa[F][a] = K1_MOFF + (ks * 4 + g) * 1536 + q * 192 + (((3 * (wn & 1) + a) ^ (g & 1)) * 32) + p * 8;
                else wa[F][a] = K1_QOFF + (ks * 4 + g) * 2048 + wn * 1024 + q * 128 + ((a ^ (((q >> 1) & 1) | ((g & 1) << 1))) * 32) + p * 8;
            }
        }
    }
    f32x4 acc[4][4], cs[4], st[4];
#pragma unroll
    for (int i = 0; i < 4; ++i) {
        cs[i] = f32x4{0.f, 0.f, 0.f, 0.f};
        st[i] = f32x4{0.f, 0.f, 0.f, 0.f};
#pragma unroll
        for (int m = 0; m < 4; ++m) acc[m][i] = f32x4{0.f, 0.f, 0.f, 0.f};
    }
    k1_loop<TYPE>(lds, dm, xa, wa, acc, cs, st, bp, bb);
    {
        WAIT_VM(0);
        lds_ptr tp = (lds_ptr)(uintptr_t)dm.dst + K1_TR + ((lane >> 2) & 3) * 128 + g * 32 + (lane & 3) * 8;
        u32x2 lo = tr_read(tp), hi = tr_read(tp + 512);
        u32x2 lo1 = tr_read(tp + 8192), hi1 = tr_read(tp + 8192 + 512);
        st16(ws, (size_t)(dm.tdst0 - ws) + lane * 16, 0, u32x4{lo[0], lo[1], hi[0], hi[1]});
        if (dm.t1) st16(ws, (size_t)(dm.tdst1 - ws) + lane * 16, 0, u32x4{lo1[0], lo1[1], hi1[0], hi1[1]});
    }
    if constexpr (TYPE == 2) {
        if ((l15 >> 2) == g) {
#pragma unroll
            for (int m = 0; m < 2; ++m) {
                const int r = l15 & 3;
                const float rq = r == 0 ? st[2 + m][0] : r == 1 ? st[2 + m][1] : r == 2 ? st[2 + m][2] : st[2 + m][3];
                const int tl = wm * 64 + (pm + m) * 16 + l15;
                const float mu = st[m][0] * (1.0f / DM);
                float var = __builtin_fmaf(-mu, mu, rq * (1.0f / DM));
                if (!(var >= mu * mu * (1.0f / 256.0f))) {
                    const u16* xr = x + (size_t)(mb * 128 + tl) * DM;
                    float a = 0.f;
                    for (int k = 0; k < DM; ++k) {
                        float d = __uint_as_float((unsigned)xr[k] << 16) - mu;
                        a = __builtin_fmaf(d, d, a);
                    }
                    var = a * (1.0f / DM);
                }
                const float r1 = __builtin_amdgcn_rsqf(var + 1e-5f);
                const float r2 = r1 * __builtin_amdgcn_rsqf(__builtin_fmaf(var * r1, r1, 1e-5f));
                lds_wrf2(lds + K1_ROWST + tl * 8, f32x2{-mu * r1, r1});
                lds_wrf2(lds + K1_ROWST + 1024 + tl * 8, f32x2{-mu * r2, r2});
            }
        }
    }
    {
        lds_ptr cp = lds + K1_COLS + (wm * 4 + wn) * 256;
#pragma unroll
        for (int i = 0; i < NT; ++i) {
            if (TYPE == 1) { if (g == 0) lds_wrf(cp + i * 64 + l15 * 4, cs[i][0]); }
            else if (l15 == 0) lds_wrf4(cp + i * 64 + g * 16, cs[i]);
        }
    }
    LGKM_BAR();
    const int tok0 = mb * 128 + wm * 64;
    lds_ptr cp = lds + K1_COLS + ((1 - wm) * 4 + wn) * 256;
    if constexpr (TYPE == 1) {
        float c[4];
#pragma unroll
        for (int i = 0; i < 4; ++i) c[i] = cs[i][0] + lds_rdf(cp + i * 64 + l15 * 4);
#pragma unroll
        for (int mp = 0; mp < 2; ++mp) {
            f32x4 sa[2][2];
#pragma unroll
            for (int m = 0; m < 2; ++m) {
                sa[m][0] = lds_rdf4(lds + K1_ROWST + (wm * 64 + (2 * mp + m) * 16 + g * 4) * 8);
                sa[m][1] = lds_rdf4(lds + K1_ROWST + (wm * 64 + (2 * mp + m) * 16 + g * 4) * 8 + 16);
            }
            const int to = (tok0 >> 3) + mp * 4 + ((g & 1) * 2 + (g >> 1));
            const unsigned vp = (unsigned)WS_V + (to * DM + (T0 - 80) * 16 + l15) * 16;
#pragma unroll
            for (int i = 0; i < 4; ++i) {
                unsigned pkd[2][2];
#pragma unroll
                for (int m = 0; m < 2; ++m) {
                    float v[4];
#pragma unroll
                    for (int r = 0; r < 4; ++r)
                        v[r] = __builtin_fmaf(acc[2 * mp + m][i][r], sa[m][r >> 1][(r & 1) * 2 + 1], sa[m][r >> 1][(r & 1) * 2] * c[i]);
                    pkd[m][0] = pk2(v[0], v[1]);
                    pkd[m][1] = pk2(v[2], v[3]);
                }
                auto s0 = __builtin_amdgcn_permlane16_swap(pkd[0][0], pkd[1][0], false, false);
                auto s1 = __builtin_amdgcn_permlane16_swap(pkd[0][1], pkd[1][1], false, false);
                if (live) st16(ws, vp, i * 256, u32x4{s0[0], s1[0], s0[1], s1[1]});
            }
        }
    } else {
        f32x2 sr[4];
        const float sc = (TYPE == 0 && T0 < 40) ? 0.22360679774997896f * 1.4426950408889634f : 1.0f;
#pragma unroll
        for (int m = 0; m < 4; ++m) {
            sr[m] = lds_rdf2(lds + K1_ROWST + (TYPE == 2 ? 1024 : 0) + (wm * 64 + (m ^ pm) * 16 + l15) * 8);
            if (TYPE == 0) sr[m] *= sc;
        }
        if constexpr (TYPE == 2) { PIN(bb[0]); PIN(bb[1]); PIN(bb[2]); }
        const unsigned op = (unsigned)(TYPE == 2 ? WS_H : WS_QK) + ((2 * T0 + (g >> 1)) * SEQ + tok0 + (g & 1) * 16 + l15) * 16;
#pragma unroll
        for (int i = 0; i < NT; ++i) {
            const f32x4 c = cs[i] + lds_rdf4(cp + i * 64 + g * 16);
            const f32x2 c01 = {c[0], c[1]}, c23 = {c[2], c[3]};
            f32x2 b01 = {0.f, 0.f}, b23 = {0.f, 0.f};
            if constexpr (TYPE == 2) {
                b01 = f32x2{bf_lo(bb[i][0]), bf_hi(bb[i][0])};
                b23 = f32x2{bf_lo(bb[i][1]), bf_hi(bb[i][1])};
            }
#pragma unroll
            for (int mp = 0; mp < 2; ++mp) {
                unsigned pkd[2][2];
#pragma unroll
                for (int m = 0; m < 2; ++m) {
                    const f32x4 a = acc[2 * mp + m][i];
                    const f32x2 nm = {sr[2 * mp + m][0], sr[2 * mp + m][0]}, rr = {sr[2 * mp + m][1], sr[2 * mp + m][1]};
                    f32x2 u0, u1;
                    if constexpr (TYPE == 2) {
                        u0 = gelu_tanh2(__builtin_elementwise_fma(f32x2{a[0], a[1]}, rr, __builtin_elementwise_fma(nm, c01, b01)));
                        u1 = gelu_tanh2(__builtin_elementwise_fma(f32x2{a[2], a[3]}, rr, __builtin_elementwise_fma(nm, c23, b23)));
                    } else {
                        u0 = __builtin_elementwise_fma(f32x2{a[0], a[1]}, rr, nm * c01);
                        u1 = __builtin_elementwise_fma(f32x2{a[2], a[3]}, rr, nm * c23);
                    }
                    pkd[m][0] = pk2(u0[0], u0[1]);
                    pkd[m][1] = pk2(u1[0], u1[1]);
                }
                auto s0 = __builtin_amdgcn_permlane16_swap(pkd[0][0], pkd[1][0], false, false);
                auto s1 = __builtin_amdgcn_permlane16_swap(pkd[0][1], pkd[1][1], false, false);
                st16(ws, op + ((2 * mp) ^ pm) * 256, i * 2 * SEQ * 16, u32x4{s0[0], s1[0], s0[1], s1[1]});
            }
        }
    }
}

__global__ __launch_bounds__(512, 2) void k1_qkv_mlp1(const u16* __restrict__ x, const u16* __restrict__ wqkv, const u16* __restrict__ w1,
                                                      const u16* __restrict__ b1, const u16* __restrict__ wo, const u16* __restrict__ w2,
                                                      char* __restrict__ ws) {
    unsigned kpre;
    asm volatile("s_load_dword %0, %1, 0x0" : "=s"(kpre) : "s"(__builtin_amdgcn_kernarg_segment_ptr()));
    SBAR();
    __shared__ __attribute__((aligned(1024))) char smem[K1_LDS];
    lds_ptr lds = (lds_ptr)smem;
    const unsigned lds0 = (unsigned)(uintptr_t)lds;
    const int lane = threadIdx.x & 63;
    const int w = __builtin_amdgcn_readfirstlane(threadIdx.x >> 6);
    const int b = blockIdx.x, xcd = b & 7, idx = b >> 3;
    const int mb = (xcd & 1) * 8 + (idx & 7), cg = (xcd >> 1) * 4 + (idx >> 3);
    const int cq = cg < 15 ? cg : 14;
    K1Dma dm;
    {
        dm.dst = lds0 + w * 1024;
        const int q8 = lane >> 3, c = lane & 7;
        const int r = 8 * w + q8;
        dm.vx = r * (DM * 2) + ((c ^ ((r >> 1) & 7)) * 16);
        const int ko = w >> 1;
        const int a = (c >> 1) ^ (((q8 >> 1) & 1) | ((ko & 1) << 1));
        dm.vq = (8 * ko + q8) * (3 * DM * 2) + (w & 1) * 128 + a * 32 + (c & 1) * 16;
        PIN(dm.vx); PIN(dm.vq);
        dm.xg = (const char*)x + (size_t)mb * 128 * DM * 2;
        dm.qg = (const char*)wqkv + cq * 256;
        dm.mg = (const char*)w1 + cg * 192;
        glds16(dm.vx, dm.xg, dm.dst);
        asm volatile("s_waitcnt lgkmcnt(0)" :: "s"(kpre));
        glds16(dm.vq, dm.qg, dm.dst + 2 * 8192);
        glds16(dm.vx, dm.xg + 64 * DM * 2, dm.dst + 8192);
        glds16(dm.vq, dm.qg + 32 * 3 * DM * 2, dm.dst + 3 * 8192);
        SBAR();
        unsigned vm[2];
#pragma unroll
        for (int i = 0; i < 2; ++i) {
            const unsigned u = (i ? 8 + (w & 3) : w) * 64 + lane;
            const unsigned k2 = (u * 683u) >> 16, rem = u - k2 * 96, row = (rem * 43u) >> 9, c2 = rem - row * 12;
            vm[i] = (8 * k2 + row) * (FF * 2) + (((c2 >> 1) ^ (k2 & 1)) * 32) + (c2 & 1) * 16;
            if (i == 0) glds16(vm[0], dm.mg, dm.dst + 4 * 8192);
            else if (w < 4) glds16(vm[1], dm.mg, dm.dst + 5 * 8192);
        }
        dm.vm0 = vm[0];
        dm.vm1 = vm[1];
        glds16(dm.vx, dm.xg + 128, dm.dst + K1_STAGE);
        glds16(dm.vx, dm.xg + 128 + 64 * DM * 2, dm.dst + K1_STAGE + 8192);
        glds16(dm.vq, dm.qg + 64 * 3 * DM * 2, dm.dst + K1_STAGE + 2 * 8192);
        glds16(dm.vq, dm.qg + 96 * 3 * DM * 2, dm.dst + K1_STAGE + 3 * 8192);
        glds16(dm.vm0, dm.mg + 64 * FF * 2, dm.dst + K1_STAGE + 4 * 8192);
        if (w < 4) glds16(dm.vm1, dm.mg + 64 * FF * 2, dm.dst + K1_STAGE + 5 * 8192);
    }
    const int pid0 = b + 256 * w, pid1 = pid0 + 2048;
    const bool t1 = pid1 < 2720;
    const int pl0 = pid0 / 10, cb0 = pid0 - pl0 * 10, pl1 = t1 ? pid1 / 10 : 0, cb1 = t1 ? pid1 - pl1 * 10 : 0;
    dm.tv = (lane >> 3) * (DM * 2) + (lane & 7) * 16;
    dm.tsrc0 = (pl0 < 80 ? (const char*)wo + (size_t)pl0 * 8 * DM * 2 : (const char*)w2 + (size_t)(pl0 - 80) * 8 * DM * 2) + cb0 * 128;
    dm.tsrc1 = (const char*)w2 + (size_t)(t1 ? pl1 - 80 : 0) * 8 * DM * 2 + cb1 * 128;
    dm.tdst0 = ws + WS_WT + ((size_t)pl0 * DM + cb0 * 64) * 16;
    dm.tdst1 = ws + WS_WT + ((size_t)pl1 * DM + cb1 * 64) * 16;
    dm.t1 = t1;
    {
        unsigned m32 = (unsigned)__builtin_amdgcn_readfirstlane((w - 4) >> 31);
        asm volatile("" : "+s"(m32));
        dm.lo = ((unsigned long long)m32 << 32) | m32;
    }

    const int wm = w >> 2, wn = (w & 3) ^ (wm << 1);
    if (wn >= 2) k1_body<2>(lds, dm, wm, wn, 3 * (2 * cg + wn - 2), x, b1, ws, mb, lane, true);
    else if (cg >= 10) k1_body<1>(lds, dm, wm, wn, 4 * (2 * cq + wn), x, b1, ws, mb, lane, cg < 15);
    else k1_body<0>(lds, dm, wm, wn, 4 * (2 * cq + wn), x, b1, ws, mb, lane, true);
    if (b == 0 && threadIdx.x < 8)
        __hip_atomic_store((__attribute__((address_space(1))) unsigned*)(ws + WS_CNT + threadIdx.x * 256), 0u, __ATOMIC_RELAXED, __HIP_MEMORY_SCOPE_AGENT);
}

constexpr int K2_SLOT = 23552, K2_VOFF = 12288;
constexpr int K2_RING = 88 * 1024;
constexpr int K2_FLAG = K2_RING + 3 * K2_SLOT;
constexpr int K2_LDS = 160 * 1024;
typedef __attribute__((address_space(1))) unsigned gu32;

template <int I, int N, class F> __device__ __forceinline__ void sfor(F&& f) {
    if constexpr (I < N) { f(IC<I>{}); sfor<I + 1, N>(f); }
}

__global__ __launch_bounds__(512, 2) void k2_attn(const u16* __restrict__ x, const u16* __restrict__ b2, char* __restrict__ ws,
                                                  u16* __restrict__ out) {
    unsigned kpre;
    asm volatile("s_load_dword %0, %1, 0x0" : "=s"(kpre) : "s"(__builtin_amdgcn_kernarg_segment_ptr()));
    SBAR();
    __shared__ __attribute__((aligned(1024))) char smem[K2_LDS];
    lds_ptr lds = (lds_ptr)smem;
    const unsigned lds0 = (unsigned)(uintptr_t)lds;
    const int tid = threadIdx.x, lane = tid & 63, r32 = lane & 31, hi = lane >> 5;
    const int w = __builtin_amdgcn_readfirstlane(tid >> 6);
    const int b = blockIdx.x, xcd = b & 7, idx = b >> 3;
    const int h = xcd * 4 + (idx >> 3), qb = idx & 7;
    const int odd = h & 1, plane0 = 5 * (h >> 1) + 2 * odd;
    const int mp = odd, xp = odd ? 0 : 2;
    const int tok = qb * 256 + w * 32 + r32;
    unsigned doff[3], dstr[3];
    unsigned long long dmask[3];
#pragma unroll
    for (int i = 0; i < 3; ++i) {
        const int n = w + 8 * i;
        const int gl = lane < 63 ? lane / 21 : 0, d = lane < 63 ? lane - gl * 21 : 0;
        doff[i] = n < 12 ? (unsigned)WS_QK + ((80 + plane0 + (n >> 2)) * SEQ + (n & 3) * 64 + lane) * 16
                         : (unsigned)WS_V + ((3 * (n - 12) + gl) * DM + 20 * h + d) * 16;
        dstr[i] = n < 12 ? 256 * 16 : 32 * DM * 16;
        dmask[i] = n < 12 ? ~0ull : n < 22 ? ~((1ull << 20) | (1ull << 41) | (3ull << 62)) : ((1ull << 41) - 1) & ~(1ull << 20);
    }
    PIN(doff[0]); PIN(doff[1]); PIN(doff[2]);
    u32x4 q0, q1;
    {
        const char* qp0 = ws + WS_QK + ((size_t)(plane0 + mp + hi) * SEQ + tok) * 16;
        const char* qp1 = ws + WS_QK + ((size_t)(plane0 + xp) * SEQ + tok) * 16;
        asm volatile("global_load_dwordx4 %0, %1, off" : "=v"(q0) : "v"(qp0) : "memory");
        asm volatile("global_load_dwordx4 %0, %1, off" : "=v"(q1) : "v"(qp1) : "memory");
    }
    asm volatile("s_waitcnt lgkmcnt(0)" :: "s"(kpre));
    auto first_chunks = [&]() {
#pragma unroll
        for (int c = 0; c < 2; ++c)
#pragma unroll
            for (int i = 0; i < 3; ++i) {
                if (i < 2 || w < 7) glds16m(doff[i], ws, lds0 + w * 1024 + K2_RING + (c + 1) * K2_SLOT + 8 * i * 1024, dmask[i]);
                doff[i] += dstr[i];
                PIN(doff[i]);
            }
    };
    first_chunks();
    SBAR();
    const int kap = (r32 & 19) | ((r32 & 4) << 1) | ((r32 & 8) >> 1);
    const int mb = qb * 4 + (xcd & 3), nb = (xcd >> 2) * 4 + (idx >> 3), th = w & 1;
    const __amdgpu_buffer_rsrc_t rs = __builtin_amdgcn_make_buffer_rsrc(ws, 0, (unsigned)WS_END, 0x00020000);
    const unsigned hoff = ((lane >> 4) * SEQ + (lane & 15)) * 16, woff = ((lane >> 4) * DM + (lane & 15)) * 16;
    u32x4 fr[54];
    unsigned hbase, wbase;
    auto ldfrag = [&](auto F_) {
        constexpr int F = decltype(F_)::v, ks = F / 9, i = F % 9;
        if constexpr (i < 5) fr[F] = __builtin_amdgcn_raw_buffer_load_b128(rs, woff, wbase + ks * 4 * DM * 16 + i * 256, 0);
        else fr[F] = __builtin_amdgcn_raw_buffer_load_b128(rs, hoff, (hbase ^ (((i - 5) >> 1) * 512)) + ks * 4 * SEQ * 16 + ((i - 5) & 1) * 256, 0);
    };
    unsigned km = K2_RING + K2_SLOT + (mp + hi) * 4096 + kap * 16, kx = K2_RING + K2_SLOT + xp * 4096 + kap * 16, vb[3];
#pragma unroll
    for (int r = 0; r < 3; ++r) vb[r] = K2_RING + K2_SLOT + K2_VOFF + (r32 > 20 ? 1008 - 336 * r : r32 * 16 + hi * (r == 2 ? 352 : 336));
    f32x4 acc[4][5];
    auto kstep = [&](auto KS_, auto&& act) {
        constexpr int ks = decltype(KS_)::v;
        sfor<0, 20>([&](auto J_) {
            constexpr int j = decltype(J_)::v, m = j / 5, i = j % 5;
            acc[m][i] = mfma16(fr[9 * ks + i], fr[9 * ks + 5 + m], ks ? acc[m][i] : f32x4{0.f, 0.f, 0.f, 0.f});
            act(J_);
            SBAR();
        });
    };
    gu32* cnt = (gu32*)(ws + WS_CNT + qb * 256);
    float mref = 0.f;
    const f32x16 zero = {0.f, 0.f, 0.f, 0.f, 0.f, 0.f, 0.f, 0.f, 0.f, 0.f, 0.f, 0.f, 0.f, 0.f, 0.f, 0.f};
    for (int att = 0;; ++att) {
        unsigned lw = lds0 + w * 1024;
        hbase = (unsigned)WS_H + (24 * w * SEQ + mb * 64 + th * 32) * 16;
        wbase = (unsigned)WS_WT + ((80 + 24 * w) * DM + nb * 80) * 16;
        asm volatile("" : "+s"(lw), "+s"(hbase), "+s"(wbase));
        auto piece = [&](int i, unsigned slot) {
            if (i < 2 || w < 7) glds16m(doff[i], ws, lw + K2_RING + slot + 8 * i * 1024, dmask[i]);
            doff[i] += dstr[i];
            PIN(doff[i]);
        };
        auto wopiece = [&](int r) {
            const unsigned p = 8 * r + w, ks = (p * 205u) >> 10, i = p - 5 * ks;
            glds16(woff, ws + WS_WT + (ks * 4 * DM + nb * 80 + i * 16) * 16, lw + r * 8192);
        };
        if (att == 0 && tid < 132) {
            const int sl = tid / 44, r = tid - sl * 44;
            unsigned one = (r & 3) == 3 ? 0u : ONES2;
            PIN(one);
            *(__attribute__((address_space(3))) u32x4*)(lds + K2_RING + sl * K2_SLOT + K2_VOFF + (r >> 2) * 1024 + ((r & 3) == 3 ? 1008 : 320 + (r & 3) * 336)) = u32x4{one, one, one, one};
        }
        if (tid == 0) *(__attribute__((address_space(3))) u32x2*)(lds + K2_FLAG) = u32x2{0u, 0u};
        asm volatile("s_waitcnt vmcnt(2) lgkmcnt(0)\n\ts_barrier" : "+v"(q0), "+v"(q1) :: "memory");
        __builtin_amdgcn_s_setprio(3);
        if (hi) q1 = u32x4{0u, 0u, 0u, 0u};
        else if (odd) { q1[0] = 0u; q1[1] = 0u; }
        else { q1[2] = 0u; q1[3] = 0u; }
        u32x4 kf0 = lds_rd128(lds + km), kf1 = lds_rd128(lds + kx), vf0, vf1, p0, p1;
        f32x16 s[2], negm, o;
        s[0] = mfma32(kf0, q0, zero);
        s[0] = mfma32(kf1, q1, s[0]);
        kf0 = lds_rd128(lds + km + 512);
        kf1 = lds_rd128(lds + kx + 512);
        vf0 = lds_rd128(lds + vb[0]);
        {
            unsigned z = 0u;
            PIN(z);
            vf1 = p1 = u32x4{z, z, z, z};
#pragma unroll
            for (int r = 0; r < 16; ++r) o[r] = __uint_as_float(z);
        }
        if (att == 0) {
            float m = s[0][0];
#pragma unroll
            for (int r = 1; r < 16; ++r) m = __builtin_fmaxf(m, s[0][r]);
            auto rr = __builtin_amdgcn_permlane32_swap(__float_as_uint(m), __float_as_uint(m), false, false);
            mref = __builtin_fmaxf(__uint_as_float(rr[0]), __uint_as_float(rr[1]));
        }
#pragma unroll
        for (int r = 0; r < 16; ++r) { negm[r] = -mref; s[0][r] -= mref; }
        PIN(negm);
        SBAR();
        auto tile = [&](auto TT_, auto KIND_, int c, unsigned step, unsigned nslot) {
            constexpr int TT = decltype(TT_)::v, KIND = decltype(KIND_)::v, CU = TT & 1, NX = CU ^ 1;
            auto extra = [&](auto G_) {
                constexpr int G = decltype(G_)::v;
                if constexpr (KIND == 0) {
                    if constexpr (G == 1 && TT == 1) wopiece(2 * c);
                    if constexpr (G == 1 && TT == 3) { if (c < 5) wopiece(2 * c + 1); }
                    if constexpr (TT >= 6 && (G & 1) && (TT - 6) * 2 + (G >> 1) < 3) piece((TT - 6) * 2 + (G >> 1), nslot);
                } else {
                    if constexpr (!(G & 1)) ldfrag(IC<(KIND - 1) * 16 + TT * 2 + (G >> 1)>{});
                    if constexpr (KIND == 1 && G == 1 && TT == 6) wopiece(11);
                    if constexpr (KIND == 1 && G == 1 && TT == 7) { if (w < 4) wopiece(12); }
                }
                SBAR();
            };
            auto soft = [&](auto G_) {
                constexpr int G = decltype(G_)::v;
                u32x4& p = G < 2 ? p0 : p1;
                float e0 = __builtin_amdgcn_exp2f(s[CU][4 * G]), e1 = __builtin_amdgcn_exp2f(s[CU][4 * G + 1]);
                PIN(e0); PIN(e1);
                SBAR();
                extra(G_);
                float e2 = __builtin_amdgcn_exp2f(s[CU][4 * G + 2]), e3 = __builtin_amdgcn_exp2f(s[CU][4 * G + 3]);
                PIN(e2); PIN(e3);
                SBAR();
                p[2 * (G & 1)] = pk2(e0, e1);
                p[2 * (G & 1) + 1] = pk2(e2, e3);
                PIN(p);
                SBAR();
            };
            if constexpr (TT == 6) {
                if constexpr (KIND == 0) asm volatile("s_waitcnt vmcnt(1)\n\ts_barrier" ::: "memory");
                else if constexpr (KIND == 1) asm volatile("s_waitcnt vmcnt(12)\n\ts_barrier" ::: "memory");
                km += step;
                kx += step;
                SBAR();
            }
            s[NX] = mfma32(kf0, q0, negm);
            kf0 = lds_rd128(lds + km + ((TT + 2) & 7) * 512);
            SBAR();
            soft(IC<0>{});
            s[NX] = mfma32(kf1, q1, s[NX]);
            kf1 = lds_rd128(lds + kx + ((TT + 2) & 7) * 512);
            SBAR();
            soft(IC<1>{});
            o = mfma32(vf1, p1, o);
            vf1 = lds_rd128(lds + vb[(4 * TT + 2) % 3] + ((4 * TT + 2) / 3) * 1024 + ((4 * TT + 2) % 3) * 336);
            SBAR();
            soft(IC<2>{});
            o = mfma32(vf0, p0, o);
            if constexpr (TT == 7) {
#pragma unroll
                for (int r = 0; r < 3; ++r) vb[r] += step;
            }
            constexpr int GN = (4 * TT + 4) & 31;
            vf0 = lds_rd128(lds + vb[GN % 3] + (GN / 3) * 1024 + (GN % 3) * 336);
            SBAR();
            soft(IC<3>{});
        };
        auto body = [&](auto KIND_, int c, unsigned step, unsigned nslot) {
            tile(IC<0>{}, KIND_, c, step, nslot); tile(IC<1>{}, KIND_, c, step, nslot); tile(IC<2>{}, KIND_, c, step, nslot);
            tile(IC<3>{}, KIND_, c, step, nslot); tile(IC<4>{}, KIND_, c, step, nslot); tile(IC<5>{}, KIND_, c, step, nslot);
            tile(IC<6>{}, KIND_, c, step, nslot); tile(IC<7>{}, KIND_, c, step, nslot);
        };
        unsigned nslot = 0;
#pragma unroll 1
        for (int c = 0; c < 6; ++c) {
            body(IC<0>{}, c, nslot == (unsigned)K2_SLOT ? (unsigned)(-2 * K2_SLOT) : (unsigned)K2_SLOT, nslot);
            nslot = nslot == 2u * K2_SLOT ? 0u : nslot + K2_SLOT;
        }
        body(IC<1>{}, 6, K2_SLOT, 0);
        body(IC<2>{}, 7, -K2_SLOT, 0);
        o = mfma32(vf1, p1, o);
        {
            auto rr = __builtin_amdgcn_permlane32_swap(__float_as_uint(o[8]), __float_as_uint(o[8]), false, false);
            const float l = __uint_as_float(rr[1]);
            const float inv = 1.0f / l;
            int ln = lane;
            PIN(ln);
            const unsigned ooff = (ln & 31) * 16 + (odd ? (ln >> 5 ? SEQ * 16 : 8) : (ln >> 5) * 8);
#pragma unroll
            for (int qd = 0; qd < 3; ++qd) {
                u32x2 v = {pk2(o[4 * qd] * inv, o[4 * qd + 1] * inv), pk2(o[4 * qd + 2] * inv, o[4 * qd + 3] * inv)};
                if (qd < 2 || ln < 32) __builtin_amdgcn_raw_buffer_store_b64(v, rs, ooff, (unsigned)WS_O + ((plane0 + qd) * SEQ + qb * 256 + w * 32) * 16, 16);
            }
            SBAR();
            bool ok = l <= 1.2676506e30f;
#pragma unroll
            for (int r = 0; r < 12; ++r) ok = ok && (__builtin_fabsf(o[r]) < __builtin_inff());
            if (!ok) *(__attribute__((address_space(3))) unsigned*)(lds + K2_FLAG) = 1u;
        }
        SBAR();
        __builtin_amdgcn_s_setprio(0);
        kstep(IC<0>{}, [&](auto) {});
        __builtin_amdgcn_s_setprio(3);
        asm volatile("s_waitcnt vmcnt(0) lgkmcnt(0)" ::: "memory");
        unsigned arr = 0u;
        if (lane == 0) arr = __hip_atomic_fetch_add((__attribute__((address_space(3))) unsigned*)(lds + K2_FLAG + 4), 1u, __ATOMIC_RELAXED, __HIP_MEMORY_SCOPE_WORKGROUP);
        bool sig = __builtin_amdgcn_readfirstlane(arr) == 7u;
        if (sig) {
            asm volatile("" ::: "memory");
            sig = att || !__builtin_amdgcn_readfirstlane(*(volatile __attribute__((address_space(3))) unsigned*)(lds + K2_FLAG));
            if (sig && lane == 0) __hip_atomic_fetch_add(cnt, 1u, __ATOMIC_RELAXED, __HIP_MEMORY_SCOPE_AGENT);
        }
        __builtin_amdgcn_s_setprio(0);
        SBAR();
        kstep(IC<1>{}, [&](auto J_) { constexpr int j = decltype(J_)::v; if constexpr (j % 2 == 0) ldfrag(IC<32 + j / 2>{}); });
        kstep(IC<2>{}, [&](auto J_) { constexpr int j = decltype(J_)::v; if constexpr (j % 3 == 0) ldfrag(IC<42 + j / 3>{}); });
        kstep(IC<3>{}, [&](auto J_) { constexpr int j = decltype(J_)::v; if constexpr (j % 3 == 0 && j < 15) ldfrag(IC<49 + j / 3>{}); });
        if (sig) {
            for (unsigned spins = 0; spins < (1u << 17); ++spins) {
                if ((unsigned)__builtin_amdgcn_readfirstlane(__hip_atomic_load(cnt, __ATOMIC_RELAXED, __HIP_MEMORY_SCOPE_AGENT)) >= 32u) break;
                __builtin_amdgcn_s_sleep(1);
            }
        }
        asm volatile("s_barrier" ::: "memory");
        if (att) break;
        const unsigned fl = __builtin_amdgcn_readfirstlane(*(__attribute__((address_space(3))) unsigned*)(lds + K2_FLAG));
        if (!fl) break;
        {
#pragma unroll
            for (int i = 0; i < 3; ++i) doff[i] -= 8 * dstr[i];
            float mx = -__builtin_inff();
            int ln = lane;
            PIN(ln);
#pragma unroll 1
            for (int t = 0; t < 64; ++t) {
                const u32x4 ka = __builtin_amdgcn_raw_buffer_load_b128(rs, ((ln >> 5) * SEQ + (ln & 31)) * 16, (unsigned)WS_QK + (80 + plane0 + mp) * SEQ * 16 + t * 512, 0);
                const u32x4 kb = __builtin_amdgcn_raw_buffer_load_b128(rs, (ln & 31) * 16, (unsigned)WS_QK + (80 + plane0 + xp) * SEQ * 16 + t * 512, 0);
                f32x16 z = mfma32(ka, q0, zero);
                z = mfma32(kb, q1, z);
#pragma unroll
                for (int r = 0; r < 16; ++r) mx = __builtin_fmaxf(mx, z[r]);
            }
            auto rr = __builtin_amdgcn_permlane32_swap(__float_as_uint(mx), __float_as_uint(mx), false, false);
            mref = __builtin_fmaxf(__uint_as_float(rr[0]), __uint_as_float(rr[1]));
        }
        WAIT_BAR(0);
        first_chunks();
    }
    int ln = lane;
    PIN(ln);
    u32x4 of[5][2], wq[5][5];
    const unsigned obase = (unsigned)WS_O + (20 * (w >> 1) * SEQ + mb * 64 + th * 32) * 16;
    const unsigned wqb = (w >> 1) * 25600 + ln * 16;
    kstep(IC<4>{}, [&](auto J_) {
        constexpr int j = decltype(J_)::v;
        if constexpr (j % 2 == 0) of[(j / 2) % 5][j / 10] = __builtin_amdgcn_raw_buffer_load_b128(rs, hoff, obase + ((j / 2) % 5) * 4 * SEQ * 16 + (j / 10) * 256, 16);
        else if constexpr (j < 16) wq[j / 10][(j / 2) % 5] = lds_rd128(lds + wqb + (j / 2) * 1024);
    });
    kstep(IC<5>{}, [&](auto J_) {
        constexpr int j = decltype(J_)::v;
        if constexpr (j < 9) wq[(8 + j) / 5][(8 + j) % 5] = lds_rd128(lds + wqb + (8 + j) * 1024);
    });
    sfor<17, 25>([&](auto J_) { constexpr int j = decltype(J_)::v; wq[j / 5][j % 5] = lds_rd128(lds + wqb + j * 1024); });
    asm volatile("s_waitcnt lgkmcnt(0)\n\ts_barrier" ::: "memory");
    unsigned eoff[3];
    u32x2 xv[3], bv[3];
#pragma unroll
    for (int r = 0; r < 3; ++r) {
        const unsigned u = 160 * w + 64 * r + (r < 2 ? ln : ln & 31), sub = u >> 6, m = (sub * 205u) >> 10, i = sub - 5 * m;
        const unsigned col = nb * 80 + i * 16 + ((u >> 4) & 3) * 4;
        eoff[r] = ((mb * 64 + m * 16 + (u & 15)) * DM + col) * 2;
        xv[r] = *(const u32x2*)((const char*)x + eoff[r]);
        bv[r] = *(const u32x2*)((const char*)b2 + col * 2);
    }
    SBAR();
    const unsigned rw = w * 20480 + ln * 16, rwa = rw + (th ^ 1) * 10240, rwb = rw + th * 10240;
    sfor<0, 25>([&](auto J_) {
        constexpr int j = decltype(J_)::v;
        if constexpr (j % 2 == 0 && j < 20) lds_wrf4(lds + rwa + (j / 2) * 1024, acc[2 + j / 10][(j / 2) % 5]);
        acc[0][j % 5] = mfma16(wq[j / 5][j % 5], of[j / 5][0], acc[0][j % 5]);
        SBAR();
    });
    sfor<0, 25>([&](auto J_) {
        constexpr int j = decltype(J_)::v, i = j < 10 ? j % 2 : 2 + (j - 10) % 3, k = j < 10 ? j / 2 : (j - 10) / 3;
        acc[1][i] = mfma16(wq[k][i], of[k][1], acc[1][i]);
        if constexpr (j % 2 == 1 && j < 10) lds_wrf4(lds + rwb + (j / 2) * 1024, acc[0][j / 2]);
        if constexpr (j == 13 || j == 16) lds_wrf4(lds + rwb + (5 + (j - 13) / 3) * 1024, acc[1][(j - 13) / 3]);
        SBAR();
    });
#pragma unroll
    for (int i = 2; i < 5; ++i) lds_wrf4(lds + rwb + (5 + i) * 1024, acc[1][i]);
    asm volatile("s_waitcnt lgkmcnt(0)\n\ts_barrier" ::: "memory");
#pragma unroll
    for (int r = 0; r < 3; ++r) {
        lds_ptr p = lds + (160 * w + 64 * r + (r < 2 ? ln : ln & 31)) * 16;
        f32x4 t[8];
#pragma unroll
        for (int k = 0; k < 8; ++k) t[k] = lds_rdf4(p + k * 20480);
        const f32x4 s = ((t[0] + t[1]) + (t[2] + t[3])) + ((t[4] + t[5]) + (t[6] + t[7]));
        u32x2 v;
        v[0] = pk2(s[0] + bf_lo(xv[r][0]) + bf_lo(bv[r][0]), s[1] + bf_hi(xv[r][0]) + bf_hi(bv[r][0]));
        v[1] = pk2(s[2] + bf_lo(xv[r][1]) + bf_lo(bv[r][1]), s[3] + bf_hi(xv[r][1]) + bf_hi(bv[r][1]));
        if (r < 2 || ln < 32) *(u32x2*)((char*)out + eoff[r]) = v;
    }
}

extern "C" void kernel_launch(void* const* d_in, const int* in_sizes, int n_in, void* d_out, int out_size, void* d_ws, size_t ws_size,
                              hipStream_t stream) {
    const u16* x = (const u16*)d_in[0];
    const u16* wqkv = (const u16*)d_in[1];
    const u16* wo = (const u16*)d_in[2];
    const u16* w1 = (const u16*)d_in[4];
    const u16* b1 = (const u16*)d_in[5];
    const u16* w2 = (const u16*)d_in[6];
    const u16* b2 = (const u16*)d_in[7];
    char* ws = (char*)d_ws;
    k1_qkv_mlp1<<<256, 512, 0, stream>>>(x, wqkv, w1, b1, wo, w2, ws);
    k2_attn<<<256, 512, 0, stream>>>(x, b2, ws, (u16*)d_out);
}
```
